```python
import torch

def _softmax(x, axis=-1):
    return torch.softmax(x, dim=axis)

def _gelu(x):
    return torch.nn.functional.gelu(x, approximate='tanh')

def _silu(x):
    return torch.nn.functional.silu(x)

def _sigmoid(x):
    return torch.sigmoid(x)

def _layernorm(x, axis=-1, eps=1e-05):
    m = x.mean(dim=axis, keepdim=True)
    v = x.var(dim=axis, keepdim=True, unbiased=False)
    return (x - m) / torch.sqrt(v + eps)

def _scatter_add(tgt, idx, src, axis=0):
    return tgt.clone().index_add_(axis, idx, src)

def _segment_sum(src, seg, n):
    out = torch.zeros((n,) + src.shape[1:], dtype=src.dtype)
    return out.index_add_(0, seg, src)

def _pad(x, pw):
    flat = tuple((v for pair in reversed(pw) for v in pair))
    return torch.nn.functional.pad(x, flat)
kernel_name = 'parallel_sum__softmax_full_mha__mixer_gmlp__prompt'

def setup_inputs():

    _w = lambda a: (lambda t: t.to(torch.bfloat16) if t.is_floating_point() else t)(torch.as_tensor(a))

    def _A_setup():
        B, S, D = (1, 2048, 448)
        return {'x': _w((lambda *s: torch.randn(*s, dtype=torch.bfloat16) * 0.05)(B, S, D)), 'wqkv': _w((lambda *s: torch.randn(*s, dtype=torch.bfloat16) * 0.2)(D, 3 * D)), 'wo': _w((lambda *s: torch.randn(*s, dtype=torch.bfloat16) * 0.1)(D, D))}
    _A_in = _A_setup()

    def _B_setup():
        B, S, D, F = (1, 2048, 448, 1792)
        Fh = F // 2
        return {'x': _w((lambda *s: torch.randn(*s, dtype=torch.bfloat16) * 0.05)(B, S, D)), 'w_in': _w((lambda *s: torch.randn(*s, dtype=torch.bfloat16) * 0.4)(D, 2 * Fh)), 'w_sp': _w(0.01 * (lambda *s: torch.randn(*s, dtype=torch.bfloat16) * 0.4)(S, S)), 'b_sp': _w(1.0 + 0.01 * (lambda *s: torch.randn(*s, dtype=torch.bfloat16) * 0.2)(S)), 'w_out': _w((lambda *s: torch.randn(*s, dtype=torch.bfloat16) * 0.2)(Fh, D))}
    _B_in = _B_setup()
    return {'A_x': _A_in['x'], 'A_wqkv': _A_in['wqkv'], 'A_wo': _A_in['wo'], 'B_x': _B_in['x'], 'B_w_in': _B_in['w_in'], 'B_w_sp': _B_in['w_sp'], 'B_b_sp': _B_in['b_sp'], 'B_w_out': _B_in['w_out']}

def reference(A_x, A_wqkv, A_wo, B_x, B_w_in, B_w_sp, B_b_sp, B_w_out):

    def _A_ref(x, wqkv, wo):
        B, S, D = x.shape
        H = 16
        Dh = D // H
        qkv = (lambda x, *d: x.permute(*d))((x @ wqkv).reshape(B, S, 3, H, Dh), 2, 0, 3, 1, 4)
        q, k, v = (qkv[0], qkv[1], qkv[2])
        scale = Dh ** (-0.5)
        scores = torch.einsum('bhqd,bhkd->bhqk', q, k) * scale
        p = _softmax(scores, axis=-1)
        o = torch.einsum('bhqk,bhkd->bhqd', p, v)
        return (lambda x, *d: x.permute(*d))(o, 0, 2, 1, 3).reshape(B, S, D) @ wo

    def _B_ref(x, w_in, w_sp, b_sp, w_out):
        B, S, D = x.shape
        F = w_in.shape[-1]
        Fh = F // 2
        z = _gelu(torch.einsum('bsd,df->bsf', x, w_in))
        z1 = z[..., :Fh].clone()
        z2 = _layernorm(z[..., Fh:], axis=-1)
        g = torch.einsum('ts,bsf->btf', w_sp, z2) + b_sp[None, :, None]
        return torch.einsum('bsf,fd->bsd', z1 * g, w_out)
    x = A_x
    xn = _layernorm(x, axis=-1)
    return x + _A_ref(xn, A_wqkv, A_wo) + _B_ref(xn, B_w_in, B_w_sp, B_b_sp, B_w_out)
```

```cpp
#include <hip/hip_runtime.h>
#include <hip/hip_bf16.h>
#include <stdint.h>
typedef unsigned short u16;
__device__ __forceinline__ float bf2f(u16 v){ return __uint_as_float(((unsigned)v)<<16); }
__device__ __forceinline__ u16 f2bf(float f){ unsigned u=__float_as_uint(f); u += 0x7fffu + ((u>>16)&1u); return (u16)(u>>16); }
constexpr int S=2048, D=448, H=16, DH=28, F=1792, FH=896;

__global__ void k_ln(const u16* x, int ld, int off, int n, u16* y, int ldy){
  int r=blockIdx.x; int t=threadIdx.x; __shared__ float red[256];
  float s=0; for(int i=t;i<n;i+=256) s+=bf2f(x[r*ld+off+i]);
  red[t]=s; __syncthreads(); for(int o=128;o;o>>=1){ if(t<o) red[t]+=red[t+o]; __syncthreads(); }
  float m=red[0]/n; __syncthreads();
  float v=0; for(int i=t;i<n;i+=256){ float d=bf2f(x[r*ld+off+i])-m; v+=d*d; }
  red[t]=v; __syncthreads(); for(int o=128;o;o>>=1){ if(t<o) red[t]+=red[t+o]; __syncthreads(); }
  float rs=rsqrtf(red[0]/n+1e-5f);
  for(int i=t;i<n;i+=256) y[r*ldy+i]=f2bf((bf2f(x[r*ld+off+i])-m)*rs);
}
__global__ void k_gemm(const u16* A,int lda,const u16* B,int ldb,u16* C,int ldc,int K,int mode,const u16* bias,const u16* z1,int ldz){
  __shared__ float As[16][17], Bs[16][17];
  int tx=threadIdx.x&15, ty=threadIdx.x>>4; int row=blockIdx.y*16+ty, col=blockIdx.x*16+tx;
  float acc=0;
  for(int k0=0;k0<K;k0+=16){ As[ty][tx]=bf2f(A[row*lda+k0+tx]); Bs[ty][tx]=bf2f(B[(k0+ty)*ldb+col]); __syncthreads();
    for(int k=0;k<16;k++) acc+=As[ty][k]*Bs[k][tx]; __syncthreads(); }
  if(mode==1){ float u=0.7978845608f*(acc+0.044715f*acc*acc*acc); acc=0.5f*acc*(1.f+tanhf(u)); }
  if(mode==2){ acc=(acc+bf2f(bias[row]))*bf2f(z1[row*ldz+col]); }
  C[row*ldc+col]=f2bf(acc);
}
__global__ void k_attn(const u16* qkv,u16* o){
  int q=blockIdx.x, h=blockIdx.y, t=threadIdx.x; __shared__ float sc[S]; __shared__ float red[256]; __shared__ float qs[DH];
  if(t<DH) qs[t]=bf2f(qkv[q*1344+h*DH+t]); __syncthreads();
  float mx=-1e30f;
  for(int k=t;k<S;k+=256){ float s=0; for(int d=0;d<DH;d++) s+=qs[d]*bf2f(qkv[k*1344+448+h*DH+d]); s*=0.1889822365f; sc[k]=s; mx=fmaxf(mx,s);}
  red[t]=mx; __syncthreads(); for(int o2=128;o2;o2>>=1){ if(t<o2) red[t]=fmaxf(red[t],red[t+o2]); __syncthreads(); }
  mx=red[0]; __syncthreads(); float sum=0;
  for(int k=t;k<S;k+=256){ float e=__expf(sc[k]-mx); sc[k]=e; sum+=e; }
  red[t]=sum; __syncthreads(); for(int o2=128;o2;o2>>=1){ if(t<o2) red[t]+=red[t+o2]; __syncthreads(); }
  sum=red[0];
  if(t<DH*8){ int d=t%DH, p=t/DH; float a=0; for(int k=p;k<S;k+=8) a+=sc[k]*bf2f(qkv[k*1344+896+h*DH+d]); __syncthreads(); red[t]=a; }
  else __syncthreads();
  __syncthreads();
  if(t<DH){ float a=0; for(int p=0;p<8;p++) a+=red[p*DH+t]; o[q*1344+h*DH+t]=f2bf(a/sum);}
}
__global__ void k_final(const u16* A,const u16* wo,const u16* wout,const u16* x,u16* out){
  __shared__ float As[16][17], Bs[16][17];
  int tx=threadIdx.x&15, ty=threadIdx.x>>4; int row=blockIdx.y*16+ty, col=blockIdx.x*16+tx;
  float acc=0;
  for(int k0=0;k0<1344;k0+=16){ As[ty][tx]=bf2f(A[row*1344+k0+tx]); int k=k0+ty; Bs[ty][tx]= k<448? bf2f(wo[k*448+col]) : bf2f(wout[(k-448)*448+col]); __syncthreads();
    for(int kk=0;kk<16;kk++) acc+=As[ty][kk]*Bs[kk][tx]; __syncthreads(); }
  out[row*448+col]=f2bf(acc+bf2f(x[row*448+col]));
}
extern "C" void kernel_launch(void* const* d_in, const int* in_sizes, int n_in, void* d_out, int out_size, void* d_ws, size_t ws_size, hipStream_t stream){
  const u16* x=(const u16*)d_in[0]; const u16* wqkv=(const u16*)d_in[1]; const u16* wo=(const u16*)d_in[2];
  const u16* win=(const u16*)d_in[4]; const u16* wsp=(const u16*)d_in[5]; const u16* bsp=(const u16*)d_in[6]; const u16* wout=(const u16*)d_in[7];
  u16* ws=(u16*)d_ws; u16* xn=ws; u16* qkv=xn+S*D; u16* z=qkv+S*1344; u16* z2n=z+S*F; u16* cat=z2n+S*FH;
  k_ln<<<S,256,0,stream>>>(x,D,0,D,xn,D);
  k_gemm<<<dim3(1344/16,S/16),256,0,stream>>>(xn,D,wqkv,1344,qkv,1344,D,0,nullptr,nullptr,0);
  k_gemm<<<dim3(F/16,S/16),256,0,stream>>>(xn,D,win,F,z,F,D,1,nullptr,nullptr,0);
  k_ln<<<S,256,0,stream>>>(z,F,FH,FH,z2n,FH);
  k_attn<<<dim3(S,H),256,0,stream>>>(qkv,cat);
  k_gemm<<<dim3(FH/16,S/16),256,0,stream>>>(wsp,S,z2n,FH,cat+448,1344,S,2,bsp,z,F);
  k_final<<<dim3(D/16,S/16),256,0,stream>>>(cat,wo,wout,x,(u16*)d_out);
}
```

```cpp
#include <hip/hip_runtime.h>
#include <stdint.h>

typedef unsigned short u16;
typedef unsigned long long u64;
typedef __attribute__((ext_vector_type(8))) short bf16x8;
typedef __attribute__((ext_vector_type(4))) short s16x4;
typedef __attribute__((ext_vector_type(16))) float f32x16;
typedef __attribute__((ext_vector_type(4))) float f32x4;
typedef __attribute__((ext_vector_type(8))) float f32x8;
typedef __attribute__((ext_vector_type(2))) float f32x2;
typedef __attribute__((ext_vector_type(4))) unsigned u32x4;
typedef __attribute__((ext_vector_type(2))) unsigned u32x2;
typedef __attribute__((ext_vector_type(2))) __bf16 bf16x2;
#define LAS __attribute__((address_space(3)))
#define DI __device__ __forceinline__
#define MFMA32(a, b, c) __builtin_amdgcn_mfma_f32_32x32x16_bf16((a), (b), (c), 0, 0, 0)
#define SBAR() __builtin_amdgcn_sched_barrier(0)


constexpr size_t WS_QF = 0;
constexpr size_t WS_KV = WS_QF + (2u << 20);
constexpr size_t WS_Z1 = WS_KV + (4u << 20);
constexpr size_t WS_Z2 = WS_Z1 + (4u << 20);
constexpr size_t WS_CAT = WS_Z2 + (4u << 20);
constexpr size_t WS_WC = WS_CAT + (6u << 20);
constexpr size_t WS_RS = WS_WC + (2u << 20);
constexpr size_t WS_ST = WS_RS + (64u << 10);
constexpr size_t WS_TK = WS_ST + (512u << 10);
constexpr size_t WS_FL = WS_ST + (640u << 10);
constexpr unsigned FLS = 128;

DI unsigned pk2(float a, float b) { f32x2 v = {a, b}; return __builtin_bit_cast(unsigned, __builtin_convertvector(v, bf16x2)); }
DI float bf_lo(unsigned u) { return __uint_as_float(u << 16); }
DI float bf_hi(unsigned u) { return __uint_as_float(u & 0xffff0000u); }
typedef short v4i16_t __attribute__((ext_vector_type(4)));
DI s16x4 trread(const LAS char* p) { return __builtin_bit_cast(s16x4, __builtin_amdgcn_ds_read_tr16_b64_v4i16((LAS v4i16_t*)p)); }
DI bf16x8 cat8(s16x4 a, s16x4 b) { return __builtin_shufflevector(a, b, 0, 1, 2, 3, 4, 5, 6, 7); }
DI float sum32(float v) {
    auto r = __builtin_amdgcn_permlane32_swap(__float_as_uint(v), __float_as_uint(v), false, false);
    return __uint_as_float(r[0]) + __uint_as_float(r[1]);
}

#define K1A_ASM \
    "s_mov_b64 exec, 1\n\t" \
    "global_atomic_add v235, v236, v235, %[tick] sc0\n\t" \
    "s_mov_b64 exec, -1\n\t" \
    "s_add_u32 m0, %[lx], 49152\n\t" \
    "s_nop 0\n\t" \
    "global_load_lds_dwordx4 v228, s[40:41]\n\t" \
    "s_add_u32 m0, %[lw], 49152\n\t" \
    "s_nop 0\n\t" \
    "global_load_lds_dwordx4 v232, s[42:43]\n\t" \
    "s_add_u32 m0, %[lx], 50176\n\t" \
    "s_nop 0\n\t" \
    "global_load_lds_dwordx4 v229, s[40:41]\n\t" \
    "s_add_u32 m0, %[lw], 50176\n\t" \
    "s_nop 0\n\t" \
    "global_load_lds_dwordx4 v233, s[42:43]\n\t" \
    "s_add_u32 m0, %[lx], 51200\n\t" \
    "s_nop 0\n\t" \
    "global_load_lds_dwordx4 v230, s[40:41]\n\t" \
    "s_add_u32 m0, %[lx], 52224\n\t" \
    "s_nop 0\n\t" \
    "global_load_lds_dwordx4 v231, s[40:41]\n\t" \
    "global_load_dword v243, v[252:253], off\n\t" \
    "v_add_u32 v228, 98304, v204\n\t" \
    "v_add_u32 v229, 98304, v205\n\t" \
    "v_add_u32 v230, 98304, v206\n\t" \
    "v_add_u32 v231, 98304, v207\n\t" \
    "v_add_u32 v252, 98304, v208\n\t" \
    "v_add_u32 v253, 98304, v209\n\t" \
    "v_add_u32 v254, 98304, v210\n\t" \
    "v_add_u32 v255, 98304, v211\n\t" \
    "s_setprio 1\n\t" \
    "s_barrier\n\t" \
    "ds_read_b128 v[64:67], v220 offset:0\n\t" \
    "ds_read_b64_tr_b16 v[176:177], v204 offset:16384\n\t" \
    "ds_read_b64_tr_b16 v[178:179], v204 offset:16896\n\t" \
    "ds_read_b64_tr_b16 v[180:181], v205 offset:16384\n\t" \
    "ds_read_b64_tr_b16 v[182:183], v205 offset:16896\n\t" \
    "ds_read_b128 v[68:71], v221 offset:0\n\t" \
    "ds_read_b64_tr_b16 v[184:185], v206 offset:16384\n\t" \
    "ds_read_b64_tr_b16 v[186:187], v206 offset:16896\n\t" \
    "ds_read_b128 v[72:75], v222 offset:0\n\t" \
    "ds_read_b128 v[76:79], v223 offset:0\n\t" \
    "s_waitcnt lgkmcnt(7)\n\t" \
    "v_mfma_f32_32x32x16_bf16 v[0:15], v[176:179], v[64:67], 0\n\t" \
    "ds_read_b64_tr_b16 v[188:189], v207 offset:16384\n\t" \
    "ds_read_b64_tr_b16 v[190:191], v207 offset:16896\n\t" \
    "v_mfma_f32_16x16x32_bf16 v[192:195], v[200:203], v[176:179], 0\n\t" \
    "s_waitcnt lgkmcnt(7)\n\t" \
    "v_mfma_f32_32x32x16_bf16 v[16:31], v[180:183], v[64:67], 0\n\t" \
    "ds_read_b64_tr_b16 v[176:177], v208 offset:16384\n\t" \
    "ds_read_b64_tr_b16 v[178:179], v208 offset:16896\n\t" \
    "v_mfma_f32_16x16x32_bf16 v[196:199], v[200:203], v[180:183], 0\n\t" \
    "s_waitcnt lgkmcnt(6)\n\t" \
    "v_mfma_f32_32x32x16_bf16 v[0:15], v[184:187], v[68:71], v[0:15]\n\t" \
    "ds_read_b64_tr_b16 v[180:181], v209 offset:16384\n\t" \
    "ds_read_b64_tr_b16 v[182:183], v209 offset:16896\n\t" \
    "s_add_u32 m0, %[lw], 81920\n\t" \
    "s_nop 0\n\t" \
    "global_load_lds_dwordx4 v232, s[46:47]\n\t" \
    "s_waitcnt lgkmcnt(4)\n\t" \
    "v_mfma_f32_32x32x16_bf16 v[16:31], v[188:191], v[68:71], v[16:31]\n\t" \
    "ds_read_b64_tr_b16 v[184:185], v210 offset:16384\n\t" \
    "ds_read_b64_tr_b16 v[186:187], v210 offset:16896\n\t" \
    "s_add_u32 m0, %[lw], 82944\n\t" \
    "s_nop 0\n\t" \
    "global_load_lds_dwordx4 v233, s[46:47]\n\t" \
    "s_add_u32 s46, s46, 229376\n\t" \
    "s_addc_u32 s47, s47, 0\n\t" \
    "s_waitcnt lgkmcnt(4)\n\t" \
    "v_mfma_f32_32x32x16_bf16 v[0:15], v[176:179], v[72:75], v[0:15]\n\t" \
    "ds_read_b64_tr_b16 v[188:189], v211 offset:16384\n\t" \
    "ds_read_b64_tr_b16 v[190:191], v211 offset:16896\n\t" \
    "s_add_u32 m0, %[lw], 90112\n\t" \
    "s_nop 0\n\t" \
    "global_load_lds_dwordx4 v232, s[46:47]\n\t" \
    "s_waitcnt lgkmcnt(4)\n\t" \
    "v_mfma_f32_32x32x16_bf16 v[16:31], v[180:183], v[72:75], v[16:31]\n\t" \
    "s_waitcnt lgkmcnt(0)\n\t" \
    "s_barrier\n\t" \
    "ds_read_b128 v[80:83], v220 offset:24576\n\t" \
    "ds_read_b64_tr_b16 v[176:177], v204 offset:40960\n\t" \
    "ds_read_b64_tr_b16 v[178:179], v204 offset:41472\n\t" \
    "ds_read_b64_tr_b16 v[180:181], v205 offset:40960\n\t" \
    "ds_read_b64_tr_b16 v[182:183], v205 offset:41472\n\t" \
    "v_mfma_f32_32x32x16_bf16 v[0:15], v[184:187], v[76:79], v[0:15]\n\t" \
    "v_mfma_f32_32x32x16_bf16 v[16:31], v[188:191], v[76:79], v[16:31]\n\t" \
    "ds_read_b128 v[84:87], v221 offset:24576\n\t" \
    "ds_read_b64_tr_b16 v[184:185], v206 offset:40960\n\t" \
    "ds_read_b64_tr_b16 v[186:187], v206 offset:41472\n\t" \
    "ds_read_b128 v[88:91], v222 offset:24576\n\t" \
    "ds_read_b128 v[92:95], v223 offset:24576\n\t" \
    "s_waitcnt lgkmcnt(7)\n\t" \
    "v_mfma_f32_32x32x16_bf16 v[0:15], v[176:179], v[80:83], v[0:15]\n\t" \
    "ds_read_b64_tr_b16 v[188:189], v207 offset:40960\n\t" \
    "ds_read_b64_tr_b16 v[190:191], v207 offset:41472\n\t" \
    "v_mfma_f32_16x16x32_bf16 v[192:195], v[200:203], v[176:179], v[192:195]\n\t" \
    "s_waitcnt lgkmcnt(7)\n\t" \
    "v_mfma_f32_32x32x16_bf16 v[16:31], v[180:183], v[80:83], v[16:31]\n\t" \
    "ds_read_b64_tr_b16 v[176:177], v208 offset:40960\n\t" \
    "ds_read_b64_tr_b16 v[178:179], v208 offset:41472\n\t" \
    "v_mfma_f32_16x16x32_bf16 v[196:199], v[200:203], v[180:183], v[196:199]\n\t" \
    "s_waitcnt lgkmcnt(6)\n\t" \
    "v_mfma_f32_32x32x16_bf16 v[0:15], v[184:187], v[84:87], v[0:15]\n\t" \
    "ds_read_b64_tr_b16 v[180:181], v209 offset:40960\n\t" \
    "ds_read_b64_tr_b16 v[182:183], v209 offset:41472\n\t" \
    "s_add_u32 m0, %[lw], 91136\n\t" \
    "s_nop 0\n\t" \
    "global_load_lds_dwordx4 v233, s[46:47]\n\t" \
    "s_add_u32 s46, s46, 229376\n\t" \
    "s_addc_u32 s47, s47, 0\n\t" \
    "s_waitcnt lgkmcnt(4)\n\t" \
    "v_mfma_f32_32x32x16_bf16 v[16:31], v[188:191], v[84:87], v[16:31]\n\t" \
    "ds_read_b64_tr_b16 v[184:185], v210 offset:40960\n\t" \
    "ds_read_b64_tr_b16 v[186:187], v210 offset:41472\n\t" \
    "s_add_u32 m0, %[lw], 98304\n\t" \
    "s_nop 0\n\t" \
    "global_load_lds_dwordx4 v232, s[46:47]\n\t" \
    "s_waitcnt lgkmcnt(4)\n\t" \
    "v_mfma_f32_32x32x16_bf16 v[0:15], v[176:179], v[88:91], v[0:15]\n\t" \
    "ds_read_b64_tr_b16 v[188:189], v211 offset:40960\n\t" \
    "ds_read_b64_tr_b16 v[190:191], v211 offset:41472\n\t" \
    "s_add_u32 m0, %[lw], 99328\n\t" \
    "s_nop 0\n\t" \
    "global_load_lds_dwordx4 v233, s[46:47]\n\t" \
    "s_add_u32 s46, s46, 229376\n\t" \
    "s_addc_u32 s47, s47, 0\n\t" \
    "s_waitcnt lgkmcnt(4)\n\t" \
    "v_mfma_f32_32x32x16_bf16 v[16:31], v[180:183], v[88:91], v[16:31]\n\t" \
    "s_waitcnt vmcnt(7)\n\t" \
    "s_waitcnt vmcnt(4)\n\t" \
    "s_waitcnt lgkmcnt(0)\n\t" \
    "s_barrier\n\t" \
    "ds_read_b128 v[96:99], v224 offset:0\n\t" \
    "ds_read_b64_tr_b16 v[176:177], v212 offset:16384\n\t" \
    "ds_read_b64_tr_b16 v[178:179], v212 offset:16896\n\t" \
    "ds_read_b64_tr_b16 v[180:181], v213 offset:16384\n\t" \
    "ds_read_b64_tr_b16 v[182:183], v213 offset:16896\n\t" \
    "v_mfma_f32_32x32x16_bf16 v[0:15], v[184:187], v[92:95], v[0:15]\n\t" \
    "v_mfma_f32_32x32x16_bf16 v[16:31], v[188:191], v[92:95], v[16:31]\n\t" \
    "ds_read_b128 v[100:103], v225 offset:0\n\t" \
    "ds_read_b64_tr_b16 v[184:185], v214 offset:16384\n\t" \
    "ds_read_b64_tr_b16 v[186:187], v214 offset:16896\n\t" \
    "ds_read_b128 v[104:107], v226 offset:0\n\t" \
    "ds_read_b128 v[108:111], v227 offset:0\n\t" \
    "s_waitcnt lgkmcnt(7)\n\t" \
    "v_mfma_f32_32x32x16_bf16 v[0:15], v[176:179], v[96:99], v[0:15]\n\t" \
    "ds_read_b64_tr_b16 v[188:189], v215 offset:16384\n\t" \
    "ds_read_b64_tr_b16 v[190:191], v215 offset:16896\n\t" \
    "v_mfma_f32_16x16x32_bf16 v[192:195], v[200:203], v[176:179], v[192:195]\n\t" \
    "s_waitcnt lgkmcnt(7)\n\t" \
    "v_mfma_f32_32x32x16_bf16 v[16:31], v[180:183], v[96:99], v[16:31]\n\t" \
    "ds_read_b64_tr_b16 v[176:177], v216 offset:16384\n\t" \
    "ds_read_b64_tr_b16 v[178:179], v216 offset:16896\n\t" \
    "v_mfma_f32_16x16x32_bf16 v[196:199], v[200:203], v[180:183], v[196:199]\n\t" \
    "s_waitcnt lgkmcnt(6)\n\t" \
    "v_mfma_f32_32x32x16_bf16 v[0:15], v[184:187], v[100:103], v[0:15]\n\t" \
    "ds_read_b64_tr_b16 v[180:181], v217 offset:16384\n\t" \
    "ds_read_b64_tr_b16 v[182:183], v217 offset:16896\n\t" \
    "s_add_u32 m0, %[lw], 106496\n\t" \
    "s_nop 0\n\t" \
    "global_load_lds_dwordx4 v232, s[46:47]\n\t" \
    "s_waitcnt lgkmcnt(4)\n\t" \
    "v_mfma_f32_32x32x16_bf16 v[16:31], v[188:191], v[100:103], v[16:31]\n\t" \
    "ds_read_b64_tr_b16 v[184:185], v218 offset:16384\n\t" \
    "ds_read_b64_tr_b16 v[186:187], v218 offset:16896\n\t" \
    "s_add_u32 m0, %[lw], 107520\n\t" \
    "s_nop 0\n\t" \
    "global_load_lds_dwordx4 v233, s[46:47]\n\t" \
    "s_add_u32 s46, s46, 229376\n\t" \
    "s_addc_u32 s47, s47, 0\n\t" \
    "s_waitcnt lgkmcnt(4)\n\t" \
    "v_mfma_f32_32x32x16_bf16 v[0:15], v[176:179], v[104:107], v[0:15]\n\t" \
    "ds_read_b64_tr_b16 v[188:189], v219 offset:16384\n\t" \
    "ds_read_b64_tr_b16 v[190:191], v219 offset:16896\n\t" \
    "s_add_u32 m0, %[lw], 114688\n\t" \
    "s_nop 0\n\t" \
    "global_load_lds_dwordx4 v232, s[46:47]\n\t" \
    "s_waitcnt lgkmcnt(4)\n\t" \
    "v_mfma_f32_32x32x16_bf16 v[16:31], v[180:183], v[104:107], v[16:31]\n\t" \
    "ds_read_b64_tr_b16 v[160:161], v228 offset:0\n\t" \
    "ds_read_b64_tr_b16 v[162:163], v228 offset:512\n\t" \
    "ds_read_b64_tr_b16 v[164:165], v229 offset:0\n\t" \
    "ds_read_b64_tr_b16 v[166:167], v229 offset:512\n\t" \
    "ds_read_b64_tr_b16 v[168:169], v230 offset:0\n\t" \
    "ds_read_b64_tr_b16 v[170:171], v230 offset:512\n\t" \
    "s_waitcnt vmcnt(5)\n\t" \
    "s_waitcnt lgkmcnt(6)\n\t" \
    "s_barrier\n\t" \
    "ds_read_b128 v[112:115], v224 offset:24576\n\t" \
    "ds_read_b64_tr_b16 v[176:177], v212 offset:40960\n\t" \
    "ds_read_b64_tr_b16 v[178:179], v212 offset:41472\n\t" \
    "ds_read_b64_tr_b16 v[180:181], v213 offset:40960\n\t" \
    "ds_read_b64_tr_b16 v[182:183], v213 offset:41472\n\t" \
    "v_mfma_f32_32x32x16_bf16 v[0:15], v[184:187], v[108:111], v[0:15]\n\t" \
    "v_mfma_f32_32x32x16_bf16 v[16:31], v[188:191], v[108:111], v[16:31]\n\t" \
    "s_waitcnt lgkmcnt(9)\n\t" \
    "v_mfma_f32_32x32x16_bf16 v[32:47], v[160:163], v[64:67], 0\n\t" \
    "ds_read_b64_tr_b16 v[172:173], v231 offset:0\n\t" \
    "ds_read_b64_tr_b16 v[174:175], v231 offset:512\n\t" \
    "v_mfma_f32_16x16x32_bf16 v[244:247], v[200:203], v[160:163], 0\n\t" \
    "ds_read_b128 v[116:119], v225 offset:24576\n\t" \
    "ds_read_b64_tr_b16 v[184:185], v214 offset:40960\n\t" \
    "ds_read_b64_tr_b16 v[186:187], v214 offset:41472\n\t" \
    "s_waitcnt lgkmcnt(12)\n\t" \
    "v_mfma_f32_32x32x16_bf16 v[48:63], v[164:167], v[64:67], 0\n\t" \
    "ds_read_b64_tr_b16 v[160:161], v252 offset:0\n\t" \
    "ds_read_b64_tr_b16 v[162:163], v252 offset:512\n\t" \
    "v_mfma_f32_16x16x32_bf16 v[248:251], v[200:203], v[164:167], 0\n\t" \
    "ds_read_b128 v[120:123], v226 offset:24576\n\t" \
    "ds_read_b128 v[124:127], v227 offset:24576\n\t" \
    "s_waitcnt lgkmcnt(11)\n\t" \
    "v_mfma_f32_32x32x16_bf16 v[0:15], v[176:179], v[112:115], v[0:15]\n\t" \
    "ds_read_b64_tr_b16 v[188:189], v215 offset:40960\n\t" \
    "ds_read_b64_tr_b16 v[190:191], v215 offset:41472\n\t" \
    "v_mfma_f32_16x16x32_bf16 v[192:195], v[200:203], v[176:179], v[192:195]\n\t" \
    "v_mfma_f32_32x32x16_bf16 v[32:47], v[168:171], v[68:71], v[32:47]\n\t" \
    "ds_read_b64_tr_b16 v[164:165], v253 offset:0\n\t" \
    "ds_read_b64_tr_b16 v[166:167], v253 offset:512\n\t" \
    "s_waitcnt lgkmcnt(13)\n\t" \
    "v_mfma_f32_32x32x16_bf16 v[16:31], v[180:183], v[112:115], v[16:31]\n\t" \
    "ds_read_b64_tr_b16 v[176:177], v216 offset:40960\n\t" \
    "ds_read_b64_tr_b16 v[178:179], v216 offset:41472\n\t" \
    "v_mfma_f32_16x16x32_bf16 v[196:199], v[200:203], v[180:183], v[196:199]\n\t" \
    "s_waitcnt lgkmcnt(13)\n\t" \
    "v_mfma_f32_32x32x16_bf16 v[48:63], v[172:175], v[68:71], v[48:63]\n\t" \
    "ds_read_b64_tr_b16 v[168:169], v254 offset:0\n\t" \
    "ds_read_b64_tr_b16 v[170:171], v254 offset:512\n\t" \
    "s_waitcnt lgkmcnt(12)\n\t" \
    "v_mfma_f32_32x32x16_bf16 v[0:15], v[184:187], v[116:119], v[0:15]\n\t" \
    "ds_read_b64_tr_b16 v[180:181], v217 offset:40960\n\t" \
    "ds_read_b64_tr_b16 v[182:183], v217 offset:41472\n\t" \
    "s_add_u32 m0, %[lw], 115712\n\t" \
    "s_nop 0\n\t" \
    "global_load_lds_dwordx4 v233, s[46:47]\n\t" \
    "s_add_u32 s46, s46, 229376\n\t" \
    "s_addc_u32 s47, s47, 0\n\t" \
    "s_waitcnt lgkmcnt(12)\n\t" \
    "v_mfma_f32_32x32x16_bf16 v[32:47], v[160:163], v[72:75], v[32:47]\n\t" \
    "ds_read_b64_tr_b16 v[172:173], v255 offset:0\n\t" \
    "ds_read_b64_tr_b16 v[174:175], v255 offset:512\n\t" \
    "s_waitcnt lgkmcnt(10)\n\t" \
    "v_mfma_f32_32x32x16_bf16 v[16:31], v[188:191], v[116:119], v[16:31]\n\t" \
    "ds_read_b64_tr_b16 v[184:185], v218 offset:40960\n\t" \
    "ds_read_b64_tr_b16 v[186:187], v218 offset:41472\n\t" \
    "s_add_u32 m0, %[lw], 122880\n\t" \
    "s_nop 0\n\t" \
    "global_load_lds_dwordx4 v232, s[46:47]\n\t" \
    "s_waitcnt lgkmcnt(10)\n\t" \
    "v_mfma_f32_32x32x16_bf16 v[48:63], v[164:167], v[72:75], v[48:63]\n\t" \
    "ds_read_b64_tr_b16 v[160:161], v228 offset:8192\n\t" \
    "ds_read_b64_tr_b16 v[162:163], v228 offset:8704\n\t" \
    "s_waitcnt lgkmcnt(10)\n\t" \
    "v_mfma_f32_32x32x16_bf16 v[0:15], v[176:179], v[120:123], v[0:15]\n\t" \
    "ds_read_b64_tr_b16 v[188:189], v219 offset:40960\n\t" \
    "ds_read_b64_tr_b16 v[190:191], v219 offset:41472\n\t" \
    "s_add_u32 m0, %[lw], 123904\n\t" \
    "s_nop 0\n\t" \
    "global_load_lds_dwordx4 v233, s[46:47]\n\t" \
    "s_add_u32 s46, s46, 229376\n\t" \
    "s_addc_u32 s47, s47, 0\n\t" \
    "s_waitcnt lgkmcnt(10)\n\t" \
    "v_mfma_f32_32x32x16_bf16 v[32:47], v[168:171], v[76:79], v[32:47]\n\t" \
    "ds_read_b64_tr_b16 v[164:165], v229 offset:8192\n\t" \
    "ds_read_b64_tr_b16 v[166:167], v229 offset:8704\n\t" \
    "s_waitcnt lgkmcnt(10)\n\t" \
    "v_mfma_f32_32x32x16_bf16 v[16:31], v[180:183], v[120:123], v[16:31]\n\t" \
    "s_waitcnt lgkmcnt(8)\n\t" \
    "v_mfma_f32_32x32x16_bf16 v[48:63], v[172:175], v[76:79], v[48:63]\n\t" \
    "ds_read_b64_tr_b16 v[168:169], v230 offset:8192\n\t" \
    "ds_read_b64_tr_b16 v[170:171], v230 offset:8704\n\t" \
    "s_waitcnt vmcnt(6)\n\t" \
    "s_waitcnt lgkmcnt(4)\n\t" \
    "s_barrier\n\t" \
    "ds_read_b128 v[128:131], v220 offset:0\n\t" \
    "ds_read_b64_tr_b16 v[176:177], v204 offset:16384\n\t" \
    "ds_read_b64_tr_b16 v[178:179], v204 offset:16896\n\t" \
    "ds_read_b64_tr_b16 v[180:181], v205 offset:16384\n\t" \
    "ds_read_b64_tr_b16 v[182:183], v205 offset:16896\n\t" \
    "v_mfma_f32_32x32x16_bf16 v[0:15], v[184:187], v[124:127], v[0:15]\n\t" \
    "v_mfma_f32_32x32x16_bf16 v[16:31], v[188:191], v[124:127], v[16:31]\n\t" \
    "v_mfma_f32_32x32x16_bf16 v[32:47], v[160:163], v[80:83], v[32:47]\n\t" \
    "ds_read_b64_tr_b16 v[172:173], v231 offset:8192\n\t" \
    "ds_read_b64_tr_b16 v[174:175], v231 offset:8704\n\t" \
    "v_mfma_f32_16x16x32_bf16 v[244:247], v[200:203], v[160:163], v[244:247]\n\t" \
    "ds_read_b128 v[132:135], v221 offset:0\n\t" \
    "ds_read_b64_tr_b16 v[184:185], v206 offset:16384\n\t" \
    "ds_read_b64_tr_b16 v[186:187], v206 offset:16896\n\t" \
    "s_waitcnt lgkmcnt(12)\n\t" \
    "v_mfma_f32_32x32x16_bf16 v[48:63], v[164:167], v[80:83], v[48:63]\n\t" \
    "ds_read_b64_tr_b16 v[160:161], v252 offset:8192\n\t" \
    "ds_read_b64_tr_b16 v[162:163], v252 offset:8704\n\t" \
    "v_mfma_f32_16x16x32_bf16 v[248:251], v[200:203], v[164:167], v[248:251]\n\t" \
    "ds_read_b128 v[136:139], v222 offset:0\n\t" \
    "ds_read_b128 v[140:143], v223 offset:0\n\t" \
    "s_waitcnt lgkmcnt(11)\n\t" \
    "v_mfma_f32_32x32x16_bf16 v[0:15], v[176:179], v[128:131], v[0:15]\n\t" \
    "ds_read_b64_tr_b16 v[188:189], v207 offset:16384\n\t" \
    "ds_read_b64_tr_b16 v[190:191], v207 offset:16896\n\t" \
    "v_mfma_f32_16x16x32_bf16 v[192:195], v[200:203], v[176:179], v[192:195]\n\t" \
    "v_mfma_f32_32x32x16_bf16 v[32:47], v[168:171], v[84:87], v[32:47]\n\t" \
    "ds_read_b64_tr_b16 v[164:165], v253 offset:8192\n\t" \
    "ds_read_b64_tr_b16 v[166:167], v253 offset:8704\n\t" \
    "s_waitcnt lgkmcnt(13)\n\t" \
    "v_mfma_f32_32x32x16_bf16 v[16:31], v[180:183], v[128:131], v[16:31]\n\t" \
    "ds_read_b64_tr_b16 v[176:177], v208 offset:16384\n\t" \
    "ds_read_b64_tr_b16 v[178:179], v208 offset:16896\n\t" \
    "v_mfma_f32_16x16x32_bf16 v[196:199], v[200:203], v[180:183], v[196:199]\n\t" \
    "s_waitcnt lgkmcnt(13)\n\t" \
    "v_mfma_f32_32x32x16_bf16 v[48:63], v[172:175], v[84:87], v[48:63]\n\t" \
    "ds_read_b64_tr_b16 v[168:169], v254 offset:8192\n\t" \
    "ds_read_b64_tr_b16 v[170:171], v254 offset:8704\n\t" \
    "s_waitcnt lgkmcnt(12)\n\t" \
    "v_mfma_f32_32x32x16_bf16 v[0:15], v[184:187], v[132:135], v[0:15]\n\t" \
    "ds_read_b64_tr_b16 v[180:181], v209 offset:16384\n\t" \
    "ds_read_b64_tr_b16 v[182:183], v209 offset:16896\n\t" \
    "s_add_u32 m0, %[lw], 131072\n\t" \
    "s_nop 0\n\t" \
    "global_load_lds_dwordx4 v232, s[46:47]\n\t" \
    "s_waitcnt lgkmcnt(12)\n\t" \
    "v_mfma_f32_32x32x16_bf16 v[32:47], v[160:163], v[88:91], v[32:47]\n\t" \
    "ds_read_b64_tr_b16 v[172:173], v255 offset:8192\n\t" \
    "ds_read_b64_tr_b16 v[174:175], v255 offset:8704\n\t" \
    "s_waitcnt lgkmcnt(10)\n\t" \
    "v_mfma_f32_32x32x16_bf16 v[16:31], v[188:191], v[132:135], v[16:31]\n\t" \
    "ds_read_b64_tr_b16 v[184:185], v210 offset:16384\n\t" \
    "ds_read_b64_tr_b16 v[186:187], v210 offset:16896\n\t" \
    "s_add_u32 m0, %[lw], 132096\n\t" \
    "s_nop 0\n\t" \
    "global_load_lds_dwordx4 v233, s[46:47]\n\t" \
    "s_add_u32 s46, s46, 229376\n\t" \
    "s_addc_u32 s47, s47, 0\n\t" \
    "s_waitcnt lgkmcnt(10)\n\t" \
    "v_mfma_f32_32x32x16_bf16 v[48:63], v[164:167], v[88:91], v[48:63]\n\t" \
    "ds_read_b64_tr_b16 v[64:65], v228 offset:16384\n\t" \
    "ds_read_b64_tr_b16 v[66:67], v228 offset:16896\n\t" \
    "s_waitcnt lgkmcnt(10)\n\t" \
    "v_mfma_f32_32x32x16_bf16 v[0:15], v[176:179], v[136:139], v[0:15]\n\t" \
    "ds_read_b64_tr_b16 v[188:189], v211 offset:16384\n\t" \
    "ds_read_b64_tr_b16 v[190:191], v211 offset:16896\n\t" \
    "s_waitcnt lgkmcnt(10)\n\t" \
    "v_mfma_f32_32x32x16_bf16 v[32:47], v[168:171], v[92:95], v[32:47]\n\t" \
    "ds_read_b64_tr_b16 v[68:69], v229 offset:16384\n\t" \
    "ds_read_b64_tr_b16 v[70:71], v229 offset:16896\n\t" \
    "s_waitcnt lgkmcnt(10)\n\t" \
    "v_mfma_f32_32x32x16_bf16 v[16:31], v[180:183], v[136:139], v[16:31]\n\t" \
    "s_waitcnt lgkmcnt(8)\n\t" \
    "v_mfma_f32_32x32x16_bf16 v[48:63], v[172:175], v[92:95], v[48:63]\n\t" \
    "ds_read_b64_tr_b16 v[72:73], v230 offset:16384\n\t" \
    "ds_read_b64_tr_b16 v[74:75], v230 offset:16896\n\t" \
    "s_waitcnt vmcnt(6)\n\t" \
    "s_waitcnt lgkmcnt(4)\n\t" \
    "s_barrier\n\t" \
    "ds_read_b128 v[144:147], v220 offset:24576\n\t" \
    "ds_read_b64_tr_b16 v[176:177], v204 offset:40960\n\t" \
    "ds_read_b64_tr_b16 v[178:179], v204 offset:41472\n\t" \
    "ds_read_b64_tr_b16 v[180:181], v205 offset:40960\n\t" \
    "ds_read_b64_tr_b16 v[182:183], v205 offset:41472\n\t" \
    "v_mfma_f32_32x32x16_bf16 v[0:15], v[184:187], v[140:143], v[0:15]\n\t" \
    "v_mfma_f32_32x32x16_bf16 v[16:31], v[188:191], v[140:143], v[16:31]\n\t" \
    "v_mfma_f32_32x32x16_bf16 v[32:47], v[64:67], v[96:99], v[32:47]\n\t" \
    "ds_read_b64_tr_b16 v[76:77], v231 offset:16384\n\t" \
    "ds_read_b64_tr_b16 v[78:79], v231 offset:16896\n\t" \
    "v_mfma_f32_16x16x32_bf16 v[244:247], v[200:203], v[64:67], v[244:247]\n\t" \
    "ds_read_b128 v[148:151], v221 offset:24576\n\t" \
    "ds_read_b64_tr_b16 v[184:185], v206 offset:40960\n\t" \
    "ds_read_b64_tr_b16 v[186:187], v206 offset:41472\n\t" \
    "s_waitcnt lgkmcnt(12)\n\t" \
    "v_mfma_f32_32x32x16_bf16 v[48:63], v[68:71], v[96:99], v[48:63]\n\t" \
    "ds_read_b64_tr_b16 v[64:65], v252 offset:16384\n\t" \
    "ds_read_b64_tr_b16 v[66:67], v252 offset:16896\n\t" \
    "v_mfma_f32_16x16x32_bf16 v[248:251], v[200:203], v[68:71], v[248:251]\n\t" \
    "ds_read_b128 v[152:155], v222 offset:24576\n\t" \
    "ds_read_b128 v[156:159], v223 offset:24576\n\t" \
    "s_waitcnt lgkmcnt(11)\n\t" \
    "v_mfma_f32_32x32x16_bf16 v[0:15], v[176:179], v[144:147], v[0:15]\n\t" \
    "ds_read_b64_tr_b16 v[188:189], v207 offset:40960\n\t" \
    "ds_read_b64_tr_b16 v[190:191], v207 offset:41472\n\t" \
    "v_mfma_f32_16x16x32_bf16 v[192:195], v[200:203], v[176:179], v[192:195]\n\t" \
    "v_mfma_f32_32x32x16_bf16 v[32:47], v[72:75], v[100:103], v[32:47]\n\t" \
    "ds_read_b64_tr_b16 v[68:69], v253 offset:16384\n\t" \
    "ds_read_b64_tr_b16 v[70:71], v253 offset:16896\n\t" \
    "s_waitcnt lgkmcnt(13)\n\t" \
    "v_mfma_f32_32x32x16_bf16 v[16:31], v[180:183], v[144:147], v[16:31]\n\t" \
    "ds_read_b64_tr_b16 v[176:177], v208 offset:40960\n\t" \
    "ds_read_b64_tr_b16 v[178:179], v208 offset:41472\n\t" \
    "v_mfma_f32_16x16x32_bf16 v[196:199], v[200:203], v[180:183], v[196:199]\n\t" \
    "s_waitcnt lgkmcnt(13)\n\t" \
    "v_mfma_f32_32x32x16_bf16 v[48:63], v[76:79], v[100:103], v[48:63]\n\t" \
    "ds_read_b64_tr_b16 v[72:73], v254 offset:16384\n\t" \
    "ds_read_b64_tr_b16 v[74:75], v254 offset:16896\n\t" \
    "s_waitcnt lgkmcnt(12)\n\t" \
    "v_mfma_f32_32x32x16_bf16 v[0:15], v[184:187], v[148:151], v[0:15]\n\t" \
    "ds_read_b64_tr_b16 v[180:181], v209 offset:40960\n\t" \
    "ds_read_b64_tr_b16 v[182:183], v209 offset:41472\n\t" \
    "s_waitcnt lgkmcnt(12)\n\t" \
    "v_mfma_f32_32x32x16_bf16 v[32:47], v[64:67], v[104:107], v[32:47]\n\t" \
    "ds_read_b64_tr_b16 v[76:77], v255 offset:16384\n\t" \
    "ds_read_b64_tr_b16 v[78:79], v255 offset:16896\n\t" \
    "s_waitcnt lgkmcnt(10)\n\t" \
    "v_mfma_f32_32x32x16_bf16 v[16:31], v[188:191], v[148:151], v[16:31]\n\t" \
    "ds_read_b64_tr_b16 v[184:185], v210 offset:40960\n\t" \
    "ds_read_b64_tr_b16 v[186:187], v210 offset:41472\n\t" \
    "s_waitcnt lgkmcnt(10)\n\t" \
    "v_mfma_f32_32x32x16_bf16 v[48:63], v[68:71], v[104:107], v[48:63]\n\t" \
    "ds_read_b64_tr_b16 v[64:65], v228 offset:24576\n\t" \
    "ds_read_b64_tr_b16 v[66:67], v228 offset:25088\n\t" \
    "s_waitcnt lgkmcnt(10)\n\t" \
    "v_mfma_f32_32x32x16_bf16 v[0:15], v[176:179], v[152:155], v[0:15]\n\t" \
    "ds_read_b64_tr_b16 v[188:189], v211 offset:40960\n\t" \
    "ds_read_b64_tr_b16 v[190:191], v211 offset:41472\n\t" \
    "s_waitcnt lgkmcnt(10)\n\t" \
    "v_mfma_f32_32x32x16_bf16 v[32:47], v[72:75], v[108:111], v[32:47]\n\t" \
    "ds_read_b64_tr_b16 v[68:69], v229 offset:24576\n\t" \
    "ds_read_b64_tr_b16 v[70:71], v229 offset:25088\n\t" \
    "s_waitcnt lgkmcnt(10)\n\t" \
    "v_mfma_f32_32x32x16_bf16 v[16:31], v[180:183], v[152:155], v[16:31]\n\t" \
    "s_waitcnt lgkmcnt(8)\n\t" \
    "v_mfma_f32_32x32x16_bf16 v[48:63], v[76:79], v[108:111], v[48:63]\n\t" \
    "ds_read_b64_tr_b16 v[72:73], v230 offset:24576\n\t" \
    "ds_read_b64_tr_b16 v[74:75], v230 offset:25088\n\t" \
    "s_waitcnt vmcnt(4)\n\t" \
    "s_waitcnt lgkmcnt(4)\n\t" \
    "s_barrier\n\t" \
    "ds_read_b128 v[160:163], v224 offset:0\n\t" \
    "ds_read_b64_tr_b16 v[176:177], v212 offset:16384\n\t" \
    "ds_read_b64_tr_b16 v[178:179], v212 offset:16896\n\t" \
    "ds_read_b64_tr_b16 v[180:181], v213 offset:16384\n\t" \
    "ds_read_b64_tr_b16 v[182:183], v213 offset:16896\n\t" \
    "v_mfma_f32_32x32x16_bf16 v[0:15], v[184:187], v[156:159], v[0:15]\n\t" \
    "v_mfma_f32_32x32x16_bf16 v[16:31], v[188:191], v[156:159], v[16:31]\n\t" \
    "v_mfma_f32_32x32x16_bf16 v[32:47], v[64:67], v[112:115], v[32:47]\n\t" \
    "ds_read_b64_tr_b16 v[76:77], v231 offset:24576\n\t" \
    "ds_read_b64_tr_b16 v[78:79], v231 offset:25088\n\t" \
    "v_mfma_f32_16x16x32_bf16 v[244:247], v[200:203], v[64:67], v[244:247]\n\t" \
    "ds_read_b128 v[164:167], v225 offset:0\n\t" \
    "ds_read_b64_tr_b16 v[184:185], v214 offset:16384\n\t" \
    "ds_read_b64_tr_b16 v[186:187], v214 offset:16896\n\t" \
    "s_waitcnt lgkmcnt(12)\n\t" \
    "v_mfma_f32_32x32x16_bf16 v[48:63], v[68:71], v[112:115], v[48:63]\n\t" \
    "ds_read_b64_tr_b16 v[64:65], v252 offset:24576\n\t" \
    "ds_read_b64_tr_b16 v[66:67], v252 offset:25088\n\t" \
    "v_mfma_f32_16x16x32_bf16 v[248:251], v[200:203], v[68:71], v[248:251]\n\t" \
    "ds_read_b128 v[168:171], v226 offset:0\n\t" \
    "ds_read_b128 v[172:175], v227 offset:0\n\t" \
    "s_waitcnt lgkmcnt(11)\n\t" \
    "v_mfma_f32_32x32x16_bf16 v[0:15], v[176:179], v[160:163], v[0:15]\n\t" \
    "ds_read_b64_tr_b16 v[188:189], v215 offset:16384\n\t" \
    "ds_read_b64_tr_b16 v[190:191], v215 offset:16896\n\t" \
    "v_mfma_f32_16x16x32_bf16 v[192:195], v[200:203], v[176:179], v[192:195]\n\t" \
    "v_mfma_f32_32x32x16_bf16 v[32:47], v[72:75], v[116:119], v[32:47]\n\t" \
    "ds_read_b64_tr_b16 v[68:69], v253 offset:24576\n\t" \
    "ds_read_b64_tr_b16 v[70:71], v253 offset:25088\n\t" \
    "s_waitcnt lgkmcnt(13)\n\t" \
    "v_mfma_f32_32x32x16_bf16 v[16:31], v[180:183], v[160:163], v[16:31]\n\t" \
    "ds_read_b64_tr_b16 v[176:177], v216 offset:16384\n\t" \
    "ds_read_b64_tr_b16 v[178:179], v216 offset:16896\n\t" \
    "v_mfma_f32_16x16x32_bf16 v[196:199], v[200:203], v[180:183], v[196:199]\n\t" \
    "s_waitcnt lgkmcnt(13)\n\t" \
    "v_mfma_f32_32x32x16_bf16 v[48:63], v[76:79], v[116:119], v[48:63]\n\t" \
    "ds_read_b64_tr_b16 v[72:73], v254 offset:24576\n\t" \
    "ds_read_b64_tr_b16 v[74:75], v254 offset:25088\n\t" \
    "s_waitcnt lgkmcnt(12)\n\t" \
    "v_mfma_f32_32x32x16_bf16 v[0:15], v[184:187], v[164:167], v[0:15]\n\t" \
    "ds_read_b64_tr_b16 v[180:181], v217 offset:16384\n\t" \
    "ds_read_b64_tr_b16 v[182:183], v217 offset:16896\n\t" \
    "s_waitcnt lgkmcnt(12)\n\t" \
    "v_mfma_f32_32x32x16_bf16 v[32:47], v[64:67], v[120:123], v[32:47]\n\t" \
    "ds_read_b64_tr_b16 v[76:77], v255 offset:24576\n\t" \
    "ds_read_b64_tr_b16 v[78:79], v255 offset:25088\n\t" \
    "s_waitcnt lgkmcnt(10)\n\t" \
    "v_mfma_f32_32x32x16_bf16 v[16:31], v[188:191], v[164:167], v[16:31]\n\t" \
    "ds_read_b64_tr_b16 v[184:185], v218 offset:16384\n\t" \
    "ds_read_b64_tr_b16 v[186:187], v218 offset:16896\n\t" \
    "v_cvt_pk_bf16_f32 v193, v192, 0\n\t" \
    "v_cvt_pk_bf16_f32 v197, v196, 0\n\t" \
    "s_waitcnt lgkmcnt(10)\n\t" \
    "v_mfma_f32_32x32x16_bf16 v[48:63], v[68:71], v[120:123], v[48:63]\n\t" \
    "s_waitcnt lgkmcnt(8)\n\t" \
    "v_mfma_f32_32x32x16_bf16 v[0:15], v[176:179], v[168:171], v[0:15]\n\t" \
    "ds_read_b64_tr_b16 v[188:189], v219 offset:16384\n\t" \
    "ds_read_b64_tr_b16 v[190:191], v219 offset:16896\n\t" \
    "v_lshlrev_b32 v194, 16, v193\n\t" \
    "v_lshlrev_b32 v198, 16, v197\n\t" \
    "v_sub_f32 v192, v192, v194\n\t" \
    "v_sub_f32 v196, v196, v198\n\t" \
    "s_waitcnt lgkmcnt(8)\n\t" \
    "v_mfma_f32_32x32x16_bf16 v[32:47], v[72:75], v[124:127], v[32:47]\n\t" \
    "s_waitcnt lgkmcnt(6)\n\t" \
    "v_mfma_f32_32x32x16_bf16 v[16:31], v[180:183], v[168:171], v[16:31]\n\t" \
    "v_cvt_pk_bf16_f32 v192, v194, v192\n\t" \
    "v_cvt_pk_bf16_f32 v196, v198, v196\n\t" \
    "s_mov_b32 exec_hi, 0\n\t" \
    "ds_write_b32 v234, v192 offset:0\n\t" \
    "ds_write_b32 v234, v196 offset:512\n\t" \
    "s_mov_b32 exec_hi, -1\n\t" \
    "s_waitcnt lgkmcnt(6)\n\t" \
    "v_mfma_f32_32x32x16_bf16 v[48:63], v[76:79], v[124:127], v[48:63]\n\t" \
    "s_waitcnt vmcnt(0)\n\t" \
    "s_waitcnt lgkmcnt(0)\n\t" \
    "v_mfma_f32_32x32x16_bf16 v[0:15], v[184:187], v[172:175], v[0:15]\n\t" \
    "v_mfma_f32_32x32x16_bf16 v[16:31], v[188:191], v[172:175], v[16:31]\n\t" \
    "s_waitcnt lgkmcnt(0)\n\t" \
    "s_barrier\n\t" \
    "ds_read_b32 v238, v238\n\t" \
    "ds_read_b128 v[176:179], v239 offset:0\n\t" \
    "ds_read_b128 v[180:183], v239 offset:512\n\t" \
    "ds_read_b32 v240, v240\n\t" \
    "s_setprio 3\n\t" \
    "s_waitcnt lgkmcnt(3)\n\t" \
    "v_mov_b32 v192, v238\n\t" \
    "v_mov_b32 v193, v238\n\t" \
    "v_mov_b32 v194, v238\n\t" \
    "v_mov_b32 v195, v238\n\t" \
    "s_waitcnt lgkmcnt(1)\n\t" \
    "s_nop 0\n\t" \
    "v_mfma_f32_32x32x16_bf16 v[0:15], v[176:179], v[192:195], v[0:15]\n\t" \
    "v_mfma_f32_32x32x16_bf16 v[16:31], v[180:183], v[192:195], v[16:31]\n\t" \
    "s_setprio 1\n\t" \
    "global_load_dword v237, v237, %[wsp]\n\t" \
    "s_waitcnt lgkmcnt(0)\n\t" \
    "v_mul_f32 v176, 0xc0135761, v240\n\t" \
    "v_mul_f32 v177, 0xbdd2d3e7, v240\n\t" \
    "v_readfirstlane_b32 %[tag], v235\n\t" \
    "v_mul_f32 v177, v177, v240\n\t" \
    "v_mov_b32 v221, 1\n\t" \
    "s_nop 4\n\t" \
    "v_mul_f32 v177, v177, v240\n\t" \
    "s_add_u32 %[tag], %[tag], 0x9E3779B9\n\t" \
    "v_mul_f32 v96, v0, v0\n\t" \
    "v_mul_f32 v97, v1, v1\n\t" \
    "v_mul_f32 v98, v2, v2\n\t" \
    "v_mul_f32 v99, v3, v3\n\t" \
    "v_mul_f32 v100, v4, v4\n\t" \
    "v_mul_f32 v101, v5, v5\n\t" \
    "v_mul_f32 v102, v6, v6\n\t" \
    "v_mul_f32 v103, v7, v7\n\t" \
    "v_mul_f32 v104, v8, v8\n\t" \
    "v_mul_f32 v105, v9, v9\n\t" \
    "v_mul_f32 v106, v10, v10\n\t" \
    "v_mul_f32 v107, v11, v11\n\t" \
    "v_mul_f32 v108, v12, v12\n\t" \
    "v_mul_f32 v109, v13, v13\n\t" \
    "v_mul_f32 v110, v14, v14\n\t" \
    "v_mul_f32 v111, v15, v15\n\t" \
    "v_mul_f32 v112, v16, v16\n\t" \
    "v_mul_f32 v113, v17, v17\n\t" \
    "v_mul_f32 v114, v18, v18\n\t" \
    "v_mul_f32 v115, v19, v19\n\t" \
    "v_mul_f32 v116, v20, v20\n\t" \
    "v_mul_f32 v117, v21, v21\n\t" \
    "v_mul_f32 v118, v22, v22\n\t" \
    "v_mul_f32 v119, v23, v23\n\t" \
    "v_mul_f32 v120, v24, v24\n\t" \
    "v_mul_f32 v121, v25, v25\n\t" \
    "v_mul_f32 v122, v26, v26\n\t" \
    "v_mul_f32 v123, v27, v27\n\t" \
    "v_mul_f32 v124, v28, v28\n\t" \
    "v_mul_f32 v125, v29, v29\n\t" \
    "v_mul_f32 v126, v30, v30\n\t" \
    "v_mul_f32 v127, v31, v31\n\t" \
    "v_fma_f32 v96, v96, v177, v176\n\t" \
    "v_fma_f32 v97, v97, v177, v176\n\t" \
    "v_fma_f32 v98, v98, v177, v176\n\t" \
    "v_fma_f32 v99, v99, v177, v176\n\t" \
    "v_fma_f32 v100, v100, v177, v176\n\t" \
    "v_fma_f32 v101, v101, v177, v176\n\t" \
    "v_fma_f32 v102, v102, v177, v176\n\t" \
    "v_fma_f32 v103, v103, v177, v176\n\t" \
    "v_fma_f32 v104, v104, v177, v176\n\t" \
    "v_fma_f32 v105, v105, v177, v176\n\t" \
    "v_fma_f32 v106, v106, v177, v176\n\t" \
    "v_fma_f32 v107, v107, v177, v176\n\t" \
    "v_fma_f32 v108, v108, v177, v176\n\t" \
    "v_fma_f32 v109, v109, v177, v176\n\t" \
    "v_fma_f32 v110, v110, v177, v176\n\t" \
    "v_fma_f32 v111, v111, v177, v176\n\t" \
    "v_fma_f32 v112, v112, v177, v176\n\t" \
    "v_fma_f32 v113, v113, v177, v176\n\t" \
    "v_fma_f32 v114, v114, v177, v176\n\t" \
    "v_fma_f32 v115, v115, v177, v176\n\t" \
    "v_fma_f32 v116, v116, v177, v176\n\t" \
    "v_fma_f32 v117, v117, v177, v176\n\t" \
    "v_fma_f32 v118, v118, v177, v176\n\t" \
    "v_fma_f32 v119, v119, v177, v176\n\t" \
    "v_fma_f32 v120, v120, v177, v176\n\t" \
    "v_fma_f32 v121, v121, v177, v176\n\t" \
    "v_fma_f32 v122, v122, v177, v176\n\t" \
    "v_fma_f32 v123, v123, v177, v176\n\t" \
    "v_fma_f32 v124, v124, v177, v176\n\t" \
    "v_fma_f32 v125, v125, v177, v176\n\t" \
    "v_fma_f32 v126, v126, v177, v176\n\t" \
    "v_fma_f32 v127, v127, v177, v176\n\t" \
    "v_mul_f32 v96, v96, v0\n\t" \
    "v_mul_f32 v97, v97, v1\n\t" \
    "v_mul_f32 v98, v98, v2\n\t" \
    "v_mul_f32 v99, v99, v3\n\t" \
    "v_mul_f32 v100, v100, v4\n\t" \
    "v_mul_f32 v101, v101, v5\n\t" \
    "v_mul_f32 v102, v102, v6\n\t" \
    "v_mul_f32 v103, v103, v7\n\t" \
    "v_mul_f32 v104, v104, v8\n\t" \
    "v_mul_f32 v105, v105, v9\n\t" \
    "v_mul_f32 v106, v106, v10\n\t" \
    "v_mul_f32 v107, v107, v11\n\t" \
    "v_mul_f32 v108, v108, v12\n\t" \
    "v_mul_f32 v109, v109, v13\n\t" \
    "v_mul_f32 v110, v110, v14\n\t" \
    "v_mul_f32 v111, v111, v15\n\t" \
    "v_mul_f32 v112, v112, v16\n\t" \
    "v_mul_f32 v113, v113, v17\n\t" \
    "v_mul_f32 v114, v114, v18\n\t" \
    "v_mul_f32 v115, v115, v19\n\t" \
    "v_mul_f32 v116, v116, v20\n\t" \
    "v_mul_f32 v117, v117, v21\n\t" \
    "v_mul_f32 v118, v118, v22\n\t" \
    "v_mul_f32 v119, v119, v23\n\t" \
    "v_mul_f32 v120, v120, v24\n\t" \
    "v_mul_f32 v121, v121, v25\n\t" \
    "v_mul_f32 v122, v122, v26\n\t" \
    "v_mul_f32 v123, v123, v27\n\t" \
    "v_mul_f32 v124, v124, v28\n\t" \
    "v_mul_f32 v125, v125, v29\n\t" \
    "v_mul_f32 v126, v126, v30\n\t" \
    "v_mul_f32 v127, v127, v31\n\t" \
    "v_exp_f32 v96, v96\n\t" \
    "v_exp_f32 v97, v97\n\t" \
    "v_exp_f32 v98, v98\n\t" \
    "v_exp_f32 v99, v99\n\t" \
    "v_exp_f32 v100, v100\n\t" \
    "v_exp_f32 v101, v101\n\t" \
    "v_exp_f32 v102, v102\n\t" \
    "v_exp_f32 v103, v103\n\t" \
    "v_exp_f32 v104, v104\n\t" \
    "v_exp_f32 v105, v105\n\t" \
    "v_exp_f32 v106, v106\n\t" \
    "v_exp_f32 v107, v107\n\t" \
    "v_exp_f32 v108, v108\n\t" \
    "v_exp_f32 v109, v109\n\t" \
    "v_exp_f32 v110, v110\n\t" \
    "v_exp_f32 v111, v111\n\t" \
    "v_exp_f32 v112, v112\n\t" \
    "v_exp_f32 v113, v113\n\t" \
    "v_exp_f32 v114, v114\n\t" \
    "v_exp_f32 v115, v115\n\t" \
    "v_exp_f32 v116, v116\n\t" \
    "v_exp_f32 v117, v117\n\t" \
    "v_exp_f32 v118, v118\n\t" \
    "v_exp_f32 v119, v119\n\t" \
    "v_exp_f32 v120, v120\n\t" \
    "v_exp_f32 v121, v121\n\t" \
    "v_exp_f32 v122, v122\n\t" \
    "v_exp_f32 v123, v123\n\t" \
    "v_exp_f32 v124, v124\n\t" \
    "v_exp_f32 v125, v125\n\t" \
    "v_exp_f32 v126, v126\n\t" \
    "v_exp_f32 v127, v127\n\t" \
    "v_add_f32 v96, 1.0, v96\n\t" \
    "v_add_f32 v97, 1.0, v97\n\t" \
    "v_add_f32 v98, 1.0, v98\n\t" \
    "v_add_f32 v99, 1.0, v99\n\t" \
    "v_add_f32 v100, 1.0, v100\n\t" \
    "v_add_f32 v101, 1.0, v101\n\t" \
    "v_add_f32 v102, 1.0, v102\n\t" \
    "v_add_f32 v103, 1.0, v103\n\t" \
    "v_add_f32 v104, 1.0, v104\n\t" \
    "v_add_f32 v105, 1.0, v105\n\t" \
    "v_add_f32 v106, 1.0, v106\n\t" \
    "v_add_f32 v107, 1.0, v107\n\t" \
    "v_add_f32 v108, 1.0, v108\n\t" \
    "v_add_f32 v109, 1.0, v109\n\t" \
    "v_add_f32 v110, 1.0, v110\n\t" \
    "v_add_f32 v111, 1.0, v111\n\t" \
    "v_add_f32 v112, 1.0, v112\n\t" \
    "v_add_f32 v113, 1.0, v113\n\t" \
    "v_add_f32 v114, 1.0, v114\n\t" \
    "v_add_f32 v115, 1.0, v115\n\t" \
    "v_add_f32 v116, 1.0, v116\n\t" \
    "v_add_f32 v117, 1.0, v117\n\t" \
    "v_add_f32 v118, 1.0, v118\n\t" \
    "v_add_f32 v119, 1.0, v119\n\t" \
    "v_add_f32 v120, 1.0, v120\n\t" \
    "v_add_f32 v121, 1.0, v121\n\t" \
    "v_add_f32 v122, 1.0, v122\n\t" \
    "v_add_f32 v123, 1.0, v123\n\t" \
    "v_add_f32 v124, 1.0, v124\n\t" \
    "v_add_f32 v125, 1.0, v125\n\t" \
    "v_add_f32 v126, 1.0, v126\n\t" \
    "v_add_f32 v127, 1.0, v127\n\t" \
    "v_rcp_f32 v96, v96\n\t" \
    "v_rcp_f32 v97, v97\n\t" \
    "v_rcp_f32 v98, v98\n\t" \
    "v_rcp_f32 v99, v99\n\t" \
    "v_rcp_f32 v100, v100\n\t" \
    "v_rcp_f32 v101, v101\n\t" \
    "v_rcp_f32 v102, v102\n\t" \
    "v_rcp_f32 v103, v103\n\t" \
    "v_rcp_f32 v104, v104\n\t" \
    "v_rcp_f32 v105, v105\n\t" \
    "v_rcp_f32 v106, v106\n\t" \
    "v_rcp_f32 v107, v107\n\t" \
    "v_rcp_f32 v108, v108\n\t" \
    "v_rcp_f32 v109, v109\n\t" \
    "v_rcp_f32 v110, v110\n\t" \
    "v_rcp_f32 v111, v111\n\t" \
    "v_rcp_f32 v112, v112\n\t" \
    "v_rcp_f32 v113, v113\n\t" \
    "v_rcp_f32 v114, v114\n\t" \
    "v_rcp_f32 v115, v115\n\t" \
    "v_rcp_f32 v116, v116\n\t" \
    "v_rcp_f32 v117, v117\n\t" \
    "v_rcp_f32 v118, v118\n\t" \
    "v_rcp_f32 v119, v119\n\t" \
    "v_rcp_f32 v120, v120\n\t" \
    "v_rcp_f32 v121, v121\n\t" \
    "v_rcp_f32 v122, v122\n\t" \
    "v_rcp_f32 v123, v123\n\t" \
    "v_rcp_f32 v124, v124\n\t" \
    "v_rcp_f32 v125, v125\n\t" \
    "v_rcp_f32 v126, v126\n\t" \
    "v_rcp_f32 v127, v127\n\t" \
    "v_mul_f32 v0, v0, v96\n\t" \
    "v_mul_f32 v1, v1, v97\n\t" \
    "v_mul_f32 v2, v2, v98\n\t" \
    "v_mul_f32 v3, v3, v99\n\t" \
    "v_mul_f32 v4, v4, v100\n\t" \
    "v_mul_f32 v5, v5, v101\n\t" \
    "v_mul_f32 v6, v6, v102\n\t" \
    "v_mul_f32 v7, v7, v103\n\t" \
    "v_mul_f32 v8, v8, v104\n\t" \
    "v_mul_f32 v9, v9, v105\n\t" \
    "v_mul_f32 v10, v10, v106\n\t" \
    "v_mul_f32 v11, v11, v107\n\t" \
    "v_mul_f32 v12, v12, v108\n\t" \
    "v_mul_f32 v13, v13, v109\n\t" \
    "v_mul_f32 v14, v14, v110\n\t" \
    "v_mul_f32 v15, v15, v111\n\t" \
    "v_mul_f32 v16, v16, v112\n\t" \
    "v_mul_f32 v17, v17, v113\n\t" \
    "v_mul_f32 v18, v18, v114\n\t" \
    "v_mul_f32 v19, v19, v115\n\t" \
    "v_mul_f32 v20, v20, v116\n\t" \
    "v_mul_f32 v21, v21, v117\n\t" \
    "v_mul_f32 v22, v22, v118\n\t" \
    "v_mul_f32 v23, v23, v119\n\t" \
    "v_mul_f32 v24, v24, v120\n\t" \
    "v_mul_f32 v25, v25, v121\n\t" \
    "v_mul_f32 v26, v26, v122\n\t" \
    "v_mul_f32 v27, v27, v123\n\t" \
    "v_mul_f32 v28, v28, v124\n\t" \
    "v_mul_f32 v29, v29, v125\n\t" \
    "v_mul_f32 v30, v30, v126\n\t" \
    "v_mul_f32 v31, v31, v127\n\t" \
    "v_add_f32 v178, v0, v4\n\t" \
    "v_mul_f32 v182, v0, v0\n\t" \
    "v_add_f32 v179, v1, v5\n\t" \
    "v_mul_f32 v183, v1, v1\n\t" \
    "v_add_f32 v180, v2, v6\n\t" \
    "v_mul_f32 v184, v2, v2\n\t" \
    "v_add_f32 v181, v3, v7\n\t" \
    "v_mul_f32 v185, v3, v3\n\t" \
    "v_fmac_f32 v182, v4, v4\n\t" \
    "v_fmac_f32 v183, v5, v5\n\t" \
    "v_fmac_f32 v184, v6, v6\n\t" \
    "v_fmac_f32 v185, v7, v7\n\t" \
    "v_add_f32 v178, v178, v8\n\t" \
    "v_fmac_f32 v182, v8, v8\n\t" \
    "v_add_f32 v179, v179, v9\n\t" \
    "v_fmac_f32 v183, v9, v9\n\t" \
    "v_add_f32 v180, v180, v10\n\t" \
    "v_fmac_f32 v184, v10, v10\n\t" \
    "v_add_f32 v181, v181, v11\n\t" \
    "v_fmac_f32 v185, v11, v11\n\t" \
    "v_add_f32 v178, v178, v12\n\t" \
    "v_fmac_f32 v182, v12, v12\n\t" \
    "v_add_f32 v179, v179, v13\n\t" \
    "v_fmac_f32 v183, v13, v13\n\t" \
    "v_add_f32 v180, v180, v14\n\t" \
    "v_fmac_f32 v184, v14, v14\n\t" \
    "v_add_f32 v181, v181, v15\n\t" \
    "v_fmac_f32 v185, v15, v15\n\t" \
    "v_add_f32 v178, v178, v16\n\t" \
    "v_fmac_f32 v182, v16, v16\n\t" \
    "v_add_f32 v179, v179, v17\n\t" \
    "v_fmac_f32 v183, v17, v17\n\t" \
    "v_add_f32 v180, v180, v18\n\t" \
    "v_fmac_f32 v184, v18, v18\n\t" \
    "v_add_f32 v181, v181, v19\n\t" \
    "v_fmac_f32 v185, v19, v19\n\t" \
    "v_add_f32 v178, v178, v20\n\t" \
    "v_fmac_f32 v182, v20, v20\n\t" \
    "v_add_f32 v179, v179, v21\n\t" \
    "v_fmac_f32 v183, v21, v21\n\t" \
    "v_add_f32 v180, v180, v22\n\t" \
    "v_fmac_f32 v184, v22, v22\n\t" \
    "v_add_f32 v181, v181, v23\n\t" \
    "v_fmac_f32 v185, v23, v23\n\t" \
    "v_add_f32 v178, v178, v24\n\t" \
    "v_fmac_f32 v182, v24, v24\n\t" \
    "v_add_f32 v179, v179, v25\n\t" \
    "v_fmac_f32 v183, v25, v25\n\t" \
    "v_add_f32 v180, v180, v26\n\t" \
    "v_fmac_f32 v184, v26, v26\n\t" \
    "v_add_f32 v181, v181, v27\n\t" \
    "v_fmac_f32 v185, v27, v27\n\t" \
    "v_add_f32 v178, v178, v28\n\t" \
    "v_fmac_f32 v182, v28, v28\n\t" \
    "v_add_f32 v179, v179, v29\n\t" \
    "v_fmac_f32 v183, v29, v29\n\t" \
    "v_add_f32 v180, v180, v30\n\t" \
    "v_fmac_f32 v184, v30, v30\n\t" \
    "v_add_f32 v181, v181, v31\n\t" \
    "v_fmac_f32 v185, v31, v31\n\t" \
    "v_add_f32 v178, v178, v179\n\t" \
    "v_add_f32 v182, v182, v183\n\t" \
    "v_add_f32 v180, v180, v181\n\t" \
    "v_add_f32 v184, v184, v185\n\t" \
    "v_add_f32 v178, v178, v180\n\t" \
    "v_add_f32 v182, v182, v184\n\t" \
    "v_mov_b32 v199, %[tag]\n\t" \
    "s_nop 0\n\t" \
    "v_permlane32_swap_b32 v178, v182\n\t" \
    "s_nop 0\n\t" \
    "v_add_f32 v198, v178, v182\n\t" \
    "global_store_dwordx2 v241, v[198:199], %[pub] sc1\n\t" \
    "s_setprio 3\n\t" \
    "ds_read_b64_tr_b16 v[96:97], v228 offset:32768\n\t" \
    "ds_read_b64_tr_b16 v[98:99], v228 offset:33280\n\t" \
    "ds_read_b64_tr_b16 v[100:101], v229 offset:32768\n\t" \
    "ds_read_b64_tr_b16 v[102:103], v229 offset:33280\n\t" \
    "ds_read_b64_tr_b16 v[104:105], v228 offset:40960\n\t" \
    "ds_read_b64_tr_b16 v[106:107], v228 offset:41472\n\t" \
    "ds_read_b64_tr_b16 v[108:109], v229 offset:40960\n\t" \
    "ds_read_b64_tr_b16 v[110:111], v229 offset:41472\n\t" \
    "s_waitcnt lgkmcnt(6)\n\t" \
    "v_mfma_f32_32x32x16_bf16 v[32:47], v[96:99], v[128:131], v[32:47]\n\t" \
    "ds_read_b64_tr_b16 v[112:113], v228 offset:49152\n\t" \
    "ds_read_b64_tr_b16 v[114:115], v228 offset:49664\n\t" \
    "v_mfma_f32_16x16x32_bf16 v[244:247], v[200:203], v[96:99], v[244:247]\n\t" \
    "s_waitcnt lgkmcnt(6)\n\t" \
    "v_mfma_f32_32x32x16_bf16 v[48:63], v[100:103], v[128:131], v[48:63]\n\t" \
    "ds_read_b64_tr_b16 v[116:117], v229 offset:49152\n\t" \
    "ds_read_b64_tr_b16 v[118:119], v229 offset:49664\n\t" \
    "v_mfma_f32_16x16x32_bf16 v[248:251], v[200:203], v[100:103], v[248:251]\n\t" \
    "s_waitcnt lgkmcnt(6)\n\t" \
    "v_mfma_f32_32x32x16_bf16 v[32:47], v[104:107], v[144:147], v[32:47]\n\t" \
    "ds_read_b64_tr_b16 v[96:97], v230 offset:32768\n\t" \
    "ds_read_b64_tr_b16 v[98:99], v230 offset:33280\n\t" \
    "v_mfma_f32_16x16x32_bf16 v[244:247], v[200:203], v[104:107], v[244:247]\n\t" \
    "s_waitcnt lgkmcnt(6)\n\t" \
    "v_mfma_f32_32x32x16_bf16 v[48:63], v[108:111], v[144:147], v[48:63]\n\t" \
    "ds_read_b64_tr_b16 v[100:101], v231 offset:32768\n\t" \
    "ds_read_b64_tr_b16 v[102:103], v231 offset:33280\n\t" \
    "v_mfma_f32_16x16x32_bf16 v[248:251], v[200:203], v[108:111], v[248:251]\n\t" \
    "s_waitcnt lgkmcnt(6)\n\t" \
    "v_mfma_f32_32x32x16_bf16 v[32:47], v[112:115], v[160:163], v[32:47]\n\t" \
    "ds_read_b64_tr_b16 v[104:105], v252 offset:32768\n\t" \
    "ds_read_b64_tr_b16 v[106:107], v252 offset:33280\n\t" \
    "v_mfma_f32_16x16x32_bf16 v[244:247], v[200:203], v[112:115], v[244:247]\n\t" \
    "s_waitcnt lgkmcnt(6)\n\t" \
    "v_mfma_f32_32x32x16_bf16 v[48:63], v[116:119], v[160:163], v[48:63]\n\t" \
    "ds_read_b64_tr_b16 v[108:109], v253 offset:32768\n\t" \
    "ds_read_b64_tr_b16 v[110:111], v253 offset:33280\n\t" \
    "v_mfma_f32_16x16x32_bf16 v[248:251], v[200:203], v[116:119], v[248:251]\n\t" \
    "s_waitcnt lgkmcnt(6)\n\t" \
    "v_mfma_f32_32x32x16_bf16 v[32:47], v[96:99], v[132:135], v[32:47]\n\t" \
    "ds_read_b64_tr_b16 v[112:113], v254 offset:32768\n\t" \
    "ds_read_b64_tr_b16 v[114:115], v254 offset:33280\n\t" \
    "s_waitcnt lgkmcnt(6)\n\t" \
    "v_mfma_f32_32x32x16_bf16 v[48:63], v[100:103], v[132:135], v[48:63]\n\t" \
    "ds_read_b64_tr_b16 v[116:117], v255 offset:32768\n\t" \
    "ds_read_b64_tr_b16 v[118:119], v255 offset:33280\n\t" \
    "s_waitcnt lgkmcnt(6)\n\t" \
    "v_mfma_f32_32x32x16_bf16 v[32:47], v[104:107], v[136:139], v[32:47]\n\t" \
    "ds_read_b64_tr_b16 v[96:97], v230 offset:40960\n\t" \
    "ds_read_b64_tr_b16 v[98:99], v230 offset:41472\n\t" \
    "v_cvt_pk_bf16_f32 v245, v244, 0\n\t" \
    "v_cvt_pk_bf16_f32 v249, v248, 0\n\t" \
    "s_waitcnt lgkmcnt(6)\n\t" \
    "v_mfma_f32_32x32x16_bf16 v[48:63], v[108:111], v[136:139], v[48:63]\n\t" \
    "ds_read_b64_tr_b16 v[100:101], v231 offset:40960\n\t" \
    "ds_read_b64_tr_b16 v[102:103], v231 offset:41472\n\t" \
    "v_lshlrev_b32 v246, 16, v245\n\t" \
    "v_lshlrev_b32 v250, 16, v249\n\t" \
    "v_sub_f32 v244, v244, v246\n\t" \
    "v_sub_f32 v248, v248, v250\n\t" \
    "s_waitcnt lgkmcnt(6)\n\t" \
    "v_mfma_f32_32x32x16_bf16 v[32:47], v[112:115], v[140:143], v[32:47]\n\t" \
    "ds_read_b64_tr_b16 v[104:105], v252 offset:40960\n\t" \
    "ds_read_b64_tr_b16 v[106:107], v252 offset:41472\n\t" \
    "v_cvt_pk_bf16_f32 v244, v246, v244\n\t" \
    "v_cvt_pk_bf16_f32 v248, v250, v248\n\t" \
    "s_mov_b32 exec_hi, 0\n\t" \
    "ds_write_b32 v234, v244 offset:1024\n\t" \
    "ds_write_b32 v234, v248 offset:1536\n\t" \
    "s_mov_b64 exec, 1\n\t" \
    "ds_add_u32 v242, v221 offset:8\n\t" \
    "s_mov_b64 exec, -1\n\t" \
    "s_waitcnt lgkmcnt(9)\n\t" \
    "v_mfma_f32_32x32x16_bf16 v[48:63], v[116:119], v[140:143], v[48:63]\n\t" \
    "ds_read_b64_tr_b16 v[108:109], v253 offset:40960\n\t" \
    "ds_read_b64_tr_b16 v[110:111], v253 offset:41472\n\t" \
    "s_waitcnt lgkmcnt(9)\n\t" \
    "v_mfma_f32_32x32x16_bf16 v[32:47], v[96:99], v[148:151], v[32:47]\n\t" \
    "ds_read_b64_tr_b16 v[112:113], v254 offset:40960\n\t" \
    "ds_read_b64_tr_b16 v[114:115], v254 offset:41472\n\t" \
    "s_waitcnt lgkmcnt(9)\n\t" \
    "v_mfma_f32_32x32x16_bf16 v[48:63], v[100:103], v[148:151], v[48:63]\n\t" \
    "ds_read_b64_tr_b16 v[116:117], v255 offset:40960\n\t" \
    "ds_read_b64_tr_b16 v[118:119], v255 offset:41472\n\t" \
    "s_waitcnt lgkmcnt(9)\n\t" \
    "v_mfma_f32_32x32x16_bf16 v[32:47], v[104:107], v[152:155], v[32:47]\n\t" \
    "ds_read_b64_tr_b16 v[96:97], v230 offset:49152\n\t" \
    "ds_read_b64_tr_b16 v[98:99], v230 offset:49664\n\t" \
    "s_waitcnt lgkmcnt(6)\n\t" \
    "v_mfma_f32_32x32x16_bf16 v[48:63], v[108:111], v[152:155], v[48:63]\n\t" \
    "ds_read_b64_tr_b16 v[100:101], v231 offset:49152\n\t" \
    "ds_read_b64_tr_b16 v[102:103], v231 offset:49664\n\t" \
    "s_waitcnt lgkmcnt(6)\n\t" \
    "v_mfma_f32_32x32x16_bf16 v[32:47], v[112:115], v[156:159], v[32:47]\n\t" \
    "ds_read_b64_tr_b16 v[104:105], v252 offset:49152\n\t" \
    "ds_read_b64_tr_b16 v[106:107], v252 offset:49664\n\t" \
    "s_waitcnt lgkmcnt(6)\n\t" \
    "v_mfma_f32_32x32x16_bf16 v[48:63], v[116:119], v[156:159], v[48:63]\n\t" \
    "ds_read_b64_tr_b16 v[108:109], v253 offset:49152\n\t" \
    "ds_read_b64_tr_b16 v[110:111], v253 offset:49664\n\t" \
    "s_waitcnt lgkmcnt(6)\n\t" \
    "v_mfma_f32_32x32x16_bf16 v[32:47], v[96:99], v[164:167], v[32:47]\n\t" \
    "ds_read_b64_tr_b16 v[112:113], v254 offset:49152\n\t" \
    "ds_read_b64_tr_b16 v[114:115], v254 offset:49664\n\t" \
    "s_waitcnt lgkmcnt(6)\n\t" \
    "v_mfma_f32_32x32x16_bf16 v[48:63], v[100:103], v[164:167], v[48:63]\n\t" \
    "ds_read_b64_tr_b16 v[116:117], v255 offset:49152\n\t" \
    "ds_read_b64_tr_b16 v[118:119], v255 offset:49664\n\t" \
    "s_waitcnt lgkmcnt(6)\n\t" \
    "v_mfma_f32_32x32x16_bf16 v[32:47], v[104:107], v[168:171], v[32:47]\n\t" \
    "s_waitcnt lgkmcnt(4)\n\t" \
    "v_mfma_f32_32x32x16_bf16 v[48:63], v[108:111], v[168:171], v[48:63]\n\t" \
    "s_waitcnt lgkmcnt(2)\n\t" \
    "v_mfma_f32_32x32x16_bf16 v[32:47], v[112:115], v[172:175], v[32:47]\n\t" \
    "s_waitcnt lgkmcnt(0)\n\t" \
    "v_mfma_f32_32x32x16_bf16 v[48:63], v[116:119], v[172:175], v[48:63]\n\t" \
    "global_load_dwordx2 v[64:65], v241, %[slots] offset:-3584 sc1\n\t" \
    "global_load_dwordx2 v[66:67], v241, %[slots] offset:-3072 sc1\n\t" \
    "global_load_dwordx2 v[68:69], v241, %[slots] offset:-2560 sc1\n\t" \
    "global_load_dwordx2 v[70:71], v241, %[slots] offset:-2048 sc1\n\t" \
    "global_load_dwordx2 v[72:73], v241, %[slots] offset:-1536 sc1\n\t" \
    "global_load_dwordx2 v[74:75], v241, %[slots] offset:-1024 sc1\n\t" \
    "global_load_dwordx2 v[76:77], v241, %[slots] offset:-512 sc1\n\t" \
    "global_load_dwordx2 v[78:79], v241, %[slots] offset:0 sc1\n\t" \
    "global_load_dwordx2 v[80:81], v241, %[slots] offset:512 sc1\n\t" \
    "global_load_dwordx2 v[82:83], v241, %[slots] offset:1024 sc1\n\t" \
    "global_load_dwordx2 v[84:85], v241, %[slots] offset:1536 sc1\n\t" \
    "global_load_dwordx2 v[86:87], v241, %[slots] offset:2048 sc1\n\t" \
    "global_load_dwordx2 v[88:89], v241, %[slots] offset:2560 sc1\n\t" \
    "global_load_dwordx2 v[90:91], v241, %[slots] offset:3072 sc1\n\t" \
    "10:\n\t" \
    "ds_read_b32 v220, v242 offset:8\n\t" \
    "s_waitcnt lgkmcnt(0)\n\t" \
    "v_readfirstlane_b32 %[tmp], v220\n\t" \
    "s_nop 1\n\t" \
    "s_cmp_lt_u32 %[tmp], 4\n\t" \
    "s_cbranch_scc1 10b\n\t" \
    "ds_read_b128 v[176:179], v239 offset:1024\n\t" \
    "ds_read_b128 v[180:183], v239 offset:1536\n\t" \
    "s_waitcnt lgkmcnt(1)\n\t" \
    "v_mfma_f32_32x32x16_bf16 v[32:47], v[176:179], v[192:195], v[32:47]\n\t" \
    "s_waitcnt lgkmcnt(0)\n\t" \
    "v_mfma_f32_32x32x16_bf16 v[48:63], v[180:183], v[192:195], v[48:63]\n\t" \
    "s_setprio 1\n\t" \
    "s_nop 10\n\t"
#define K1A_CLOB "v96", "v97", "v98", "v99", "v100", "v101", "v102", "v103", "v104", "v105", "v106", "v107", "v108", "v109", "v110", "v111", "v112", "v113", "v114", "v115", "v116", "v117", "v118", "v119", "v120", "v121", "v122", "v123", "v124", "v125", "v126", "v127", "v128", "v129", "v130", "v131", "v132", "v133", "v134", "v135", "v136", "v137", "v138", "v139", "v140", "v141", "v142", "v143", "v144", "v145", "v146", "v147", "v148", "v149", "v150", "v151", "v152", "v153", "v154", "v155", "v156", "v157", "v158", "v159", "v160", "v161", "v162", "v163", "v164", "v165", "v166", "v167", "v168", "v169", "v170", "v171", "v172", "v173", "v174", "v175", "v176", "v177", "v178", "v179", "v180", "v181", "v182", "v183", "v184", "v185", "v186", "v187", "v188", "v189", "v190", "v191", "v192", "v193", "v194", "v195", "v196", "v197", "v198", "v199", "v244", "v245", "v246", "v247", "v248", "v249", "v250", "v251", "v254", "v255"
#define K1B0_ASM \
    "s_add_u32 m0, %[lx], 0\n\t" \
    "s_nop 0\n\t" \
    "global_load_lds_dwordx4 v216, s[40:41]\n\t" \
    "s_add_u32 m0, %[lw], 0\n\t" \
    "s_nop 0\n\t" \
    "global_load_lds_dwordx4 v220, s[42:43]\n\t" \
    "s_add_u32 m0, %[lx], 1024\n\t" \
    "s_nop 0\n\t" \
    "global_load_lds_dwordx4 v217, s[40:41]\n\t" \
    "s_add_u32 m0, %[lw], 1024\n\t" \
    "s_nop 0\n\t" \
    "global_load_lds_dwordx4 v221, s[42:43]\n\t" \
    "s_add_u32 m0, %[lx], 2048\n\t" \
    "s_nop 0\n\t" \
    "global_load_lds_dwordx4 v218, s[40:41]\n\t" \
    "s_add_u32 m0, %[lx], 3072\n\t" \
    "s_nop 0\n\t" \
    "global_load_lds_dwordx4 v219, s[40:41]\n\t" \
    "s_add_u32 s40, s40, 128\n\t" \
    "s_addc_u32 s41, s41, 0\n\t" \
    "s_add_u32 s42, s42, 229376\n\t" \
    "s_addc_u32 s43, s43, 0\n\t" \
    "s_add_u32 m0, %[lx], 24576\n\t" \
    "s_nop 0\n\t" \
    "global_load_lds_dwordx4 v216, s[40:41]\n\t" \
    "s_add_u32 m0, %[lw], 24576\n\t" \
    "s_nop 0\n\t" \
    "global_load_lds_dwordx4 v220, s[42:43]\n\t" \
    "s_add_u32 m0, %[lx], 25600\n\t" \
    "s_nop 0\n\t" \
    "global_load_lds_dwordx4 v217, s[40:41]\n\t" \
    "s_add_u32 m0, %[lw], 25600\n\t" \
    "s_nop 0\n\t" \
    "global_load_lds_dwordx4 v221, s[42:43]\n\t" \
    "s_add_u32 m0, %[lx], 26624\n\t" \
    "s_nop 0\n\t" \
    "global_load_lds_dwordx4 v218, s[40:41]\n\t" \
    "s_add_u32 m0, %[lx], 27648\n\t" \
    "s_nop 0\n\t" \
    "global_load_lds_dwordx4 v219, s[40:41]\n\t" \
    "s_add_u32 s40, s40, 128\n\t" \
    "s_addc_u32 s41, s41, 0\n\t" \
    "s_add_u32 s42, s42, 229376\n\t" \
    "s_addc_u32 s43, s43, 0\n\t"
#define K1B1_ASM \
    "v_mov_b32 v204, 0\n\t" \
    "v_mov_b32 v205, 0\n\t" \
    "v_mov_b32 v206, 0\n\t" \
    "v_mov_b32 v207, 0\n\t" \
    "s_add_u32 s40, s40, 128\n\t" \
    "s_addc_u32 s41, s41, 0\n\t" \
    "s_add_u32 s42, s42, 229376\n\t" \
    "s_addc_u32 s43, s43, 0\n\t" \
    "s_waitcnt vmcnt(6)\n\t" \
    "s_barrier\n\t" \
    "ds_read_b128 v[64:67], v208 offset:0\n\t" \
    "ds_read_b128 v[68:71], v209 offset:0\n\t" \
    "ds_read_b128 v[72:75], v210 offset:0\n\t" \
    "ds_read_b128 v[76:79], v211 offset:0\n\t" \
    "s_add_u32 m0, %[lx], 73728\n\t" \
    "s_nop 0\n\t" \
    "global_load_lds_dwordx4 v216, s[40:41]\n\t" \
    "s_waitcnt lgkmcnt(3)\n\t" \
    "v_mfma_f32_16x16x32_bf16 v[200:203], v[192:195], v[64:67], 0\n\t" \
    "v_lshlrev_b32 v176, 16, v64\n\t" \
    "v_and_b32 v177, 0xffff0000, v64\n\t" \
    "v_lshlrev_b32 v178, 16, v65\n\t" \
    "v_and_b32 v179, 0xffff0000, v65\n\t" \
    "v_lshlrev_b32 v180, 16, v66\n\t" \
    "v_and_b32 v181, 0xffff0000, v66\n\t" \
    "v_lshlrev_b32 v182, 16, v67\n\t" \
    "v_and_b32 v183, 0xffff0000, v67\n\t" \
    "s_add_u32 m0, %[lw], 73728\n\t" \
    "s_nop 0\n\t" \
    "global_load_lds_dwordx4 v220, s[42:43]\n\t" \
    "v_fmac_f32 v204, v176, v176\n\t" \
    "v_fmac_f32 v205, v177, v177\n\t" \
    "v_fmac_f32 v206, v178, v178\n\t" \
    "v_fmac_f32 v207, v179, v179\n\t" \
    "v_fmac_f32 v204, v180, v180\n\t" \
    "v_fmac_f32 v205, v181, v181\n\t" \
    "v_fmac_f32 v206, v182, v182\n\t" \
    "v_fmac_f32 v207, v183, v183\n\t" \
    "s_add_u32 m0, %[lx], 74752\n\t" \
    "s_nop 0\n\t" \
    "global_load_lds_dwordx4 v217, s[40:41]\n\t" \
    "s_waitcnt lgkmcnt(2)\n\t" \
    "v_mfma_f32_16x16x32_bf16 v[200:203], v[192:195], v[68:71], v[200:203]\n\t" \
    "v_lshlrev_b32 v184, 16, v68\n\t" \
    "v_and_b32 v185, 0xffff0000, v68\n\t" \
    "v_lshlrev_b32 v186, 16, v69\n\t" \
    "v_and_b32 v187, 0xffff0000, v69\n\t" \
    "v_lshlrev_b32 v188, 16, v70\n\t" \
    "v_and_b32 v189, 0xffff0000, v70\n\t" \
    "v_lshlrev_b32 v190, 16, v71\n\t" \
    "v_and_b32 v191, 0xffff0000, v71\n\t" \
    "s_add_u32 m0, %[lw], 74752\n\t" \
    "s_nop 0\n\t" \
    "global_load_lds_dwordx4 v221, s[42:43]\n\t" \
    "v_fmac_f32 v204, v184, v184\n\t" \
    "v_fmac_f32 v205, v185, v185\n\t" \
    "v_fmac_f32 v206, v186, v186\n\t" \
    "v_fmac_f32 v207, v187, v187\n\t" \
    "v_fmac_f32 v204, v188, v188\n\t" \
    "v_fmac_f32 v205, v189, v189\n\t" \
    "v_fmac_f32 v206, v190, v190\n\t" \
    "v_fmac_f32 v207, v191, v191\n\t" \
    "s_add_u32 m0, %[lx], 75776\n\t" \
    "s_nop 0\n\t" \
    "global_load_lds_dwordx4 v218, s[40:41]\n\t" \
    "s_waitcnt lgkmcnt(1)\n\t" \
    "v_mfma_f32_16x16x32_bf16 v[200:203], v[192:195], v[72:75], v[200:203]\n\t" \
    "v_lshlrev_b32 v176, 16, v72\n\t" \
    "v_and_b32 v177, 0xffff0000, v72\n\t" \
    "v_lshlrev_b32 v178, 16, v73\n\t" \
    "v_and_b32 v179, 0xffff0000, v73\n\t" \
    "v_lshlrev_b32 v180, 16, v74\n\t" \
    "v_and_b32 v181, 0xffff0000, v74\n\t" \
    "v_lshlrev_b32 v182, 16, v75\n\t" \
    "v_and_b32 v183, 0xffff0000, v75\n\t" \
    "s_add_u32 m0, %[lx], 76800\n\t" \
    "s_nop 0\n\t" \
    "global_load_lds_dwordx4 v219, s[40:41]\n\t" \
    "s_add_u32 s40, s40, 128\n\t" \
    "s_addc_u32 s41, s41, 0\n\t" \
    "s_add_u32 s42, s42, 229376\n\t" \
    "s_addc_u32 s43, s43, 0\n\t" \
    "v_fmac_f32 v204, v176, v176\n\t" \
    "v_fmac_f32 v205, v177, v177\n\t" \
    "v_fmac_f32 v206, v178, v178\n\t" \
    "v_fmac_f32 v207, v179, v179\n\t" \
    "v_fmac_f32 v204, v180, v180\n\t" \
    "v_fmac_f32 v205, v181, v181\n\t" \
    "v_fmac_f32 v206, v182, v182\n\t" \
    "v_fmac_f32 v207, v183, v183\n\t" \
    "s_waitcnt lgkmcnt(0)\n\t" \
    "v_mfma_f32_16x16x32_bf16 v[200:203], v[192:195], v[76:79], v[200:203]\n\t" \
    "v_lshlrev_b32 v184, 16, v76\n\t" \
    "v_and_b32 v185, 0xffff0000, v76\n\t" \
    "v_lshlrev_b32 v186, 16, v77\n\t" \
    "v_and_b32 v187, 0xffff0000, v77\n\t" \
    "v_lshlrev_b32 v188, 16, v78\n\t" \
    "v_and_b32 v189, 0xffff0000, v78\n\t" \
    "v_lshlrev_b32 v190, 16, v79\n\t" \
    "v_and_b32 v191, 0xffff0000, v79\n\t" \
    "v_fmac_f32 v204, v184, v184\n\t" \
    "v_fmac_f32 v205, v185, v185\n\t" \
    "v_fmac_f32 v206, v186, v186\n\t" \
    "v_fmac_f32 v207, v187, v187\n\t" \
    "v_fmac_f32 v204, v188, v188\n\t" \
    "v_fmac_f32 v205, v189, v189\n\t" \
    "v_fmac_f32 v206, v190, v190\n\t" \
    "v_fmac_f32 v207, v191, v191\n\t" \
    "s_waitcnt vmcnt(6)\n\t" \
    "s_barrier\n\t" \
    "ds_read_b128 v[80:83], v208 offset:24576\n\t" \
    "ds_read_b128 v[84:87], v209 offset:24576\n\t" \
    "ds_read_b128 v[88:91], v210 offset:24576\n\t" \
    "ds_read_b128 v[92:95], v211 offset:24576\n\t" \
    "s_add_u32 m0, %[lx], 0\n\t" \
    "s_nop 0\n\t" \
    "global_load_lds_dwordx4 v216, s[40:41]\n\t" \
    "s_waitcnt lgkmcnt(3)\n\t" \
    "v_mfma_f32_16x16x32_bf16 v[200:203], v[192:195], v[80:83], v[200:203]\n\t" \
    "v_lshlrev_b32 v176, 16, v80\n\t" \
    "v_and_b32 v177, 0xffff0000, v80\n\t" \
    "v_lshlrev_b32 v178, 16, v81\n\t" \
    "v_and_b32 v179, 0xffff0000, v81\n\t" \
    "v_lshlrev_b32 v180, 16, v82\n\t" \
    "v_and_b32 v181, 0xffff0000, v82\n\t" \
    "v_lshlrev_b32 v182, 16, v83\n\t" \
    "v_and_b32 v183, 0xffff0000, v83\n\t" \
    "s_add_u32 m0, %[lw], 0\n\t" \
    "s_nop 0\n\t" \
    "global_load_lds_dwordx4 v220, s[42:43]\n\t" \
    "v_fmac_f32 v204, v176, v176\n\t" \
    "v_fmac_f32 v205, v177, v177\n\t" \
    "v_fmac_f32 v206, v178, v178\n\t" \
    "v_fmac_f32 v207, v179, v179\n\t" \
    "v_fmac_f32 v204, v180, v180\n\t" \
    "v_fmac_f32 v205, v181, v181\n\t" \
    "v_fmac_f32 v206, v182, v182\n\t" \
    "v_fmac_f32 v207, v183, v183\n\t" \
    "s_add_u32 m0, %[lx], 1024\n\t" \
    "s_nop 0\n\t" \
    "global_load_lds_dwordx4 v217, s[40:41]\n\t" \
    "s_waitcnt lgkmcnt(2)\n\t" \
    "v_mfma_f32_16x16x32_bf16 v[200:203], v[192:195], v[84:87], v[200:203]\n\t" \
    "v_lshlrev_b32 v184, 16, v84\n\t" \
    "v_and_b32 v185, 0xffff0000, v84\n\t" \
    "v_lshlrev_b32 v186, 16, v85\n\t" \
    "v_and_b32 v187, 0xffff0000, v85\n\t" \
    "v_lshlrev_b32 v188, 16, v86\n\t" \
    "v_and_b32 v189, 0xffff0000, v86\n\t" \
    "v_lshlrev_b32 v190, 16, v87\n\t" \
    "v_and_b32 v191, 0xffff0000, v87\n\t" \
    "s_add_u32 m0, %[lw], 1024\n\t" \
    "s_nop 0\n\t" \
    "global_load_lds_dwordx4 v221, s[42:43]\n\t" \
    "v_fmac_f32 v204, v184, v184\n\t" \
    "v_fmac_f32 v205, v185, v185\n\t" \
    "v_fmac_f32 v206, v186, v186\n\t" \
    "v_fmac_f32 v207, v187, v187\n\t" \
    "v_fmac_f32 v204, v188, v188\n\t" \
    "v_fmac_f32 v205, v189, v189\n\t" \
    "v_fmac_f32 v206, v190, v190\n\t" \
    "v_fmac_f32 v207, v191, v191\n\t" \
    "s_add_u32 m0, %[lx], 2048\n\t" \
    "s_nop 0\n\t" \
    "global_load_lds_dwordx4 v218, s[40:41]\n\t" \
    "s_waitcnt lgkmcnt(1)\n\t" \
    "v_mfma_f32_16x16x32_bf16 v[200:203], v[192:195], v[88:91], v[200:203]\n\t" \
    "v_lshlrev_b32 v176, 16, v88\n\t" \
    "v_and_b32 v177, 0xffff0000, v88\n\t" \
    "v_lshlrev_b32 v178, 16, v89\n\t" \
    "v_and_b32 v179, 0xffff0000, v89\n\t" \
    "v_lshlrev_b32 v180, 16, v90\n\t" \
    "v_and_b32 v181, 0xffff0000, v90\n\t" \
    "v_lshlrev_b32 v182, 16, v91\n\t" \
    "v_and_b32 v183, 0xffff0000, v91\n\t" \
    "s_add_u32 m0, %[lx], 3072\n\t" \
    "s_nop 0\n\t" \
    "global_load_lds_dwordx4 v219, s[40:41]\n\t" \
    "s_add_u32 s40, s40, 128\n\t" \
    "s_addc_u32 s41, s41, 0\n\t" \
    "s_add_u32 s42, s42, 229376\n\t" \
    "s_addc_u32 s43, s43, 0\n\t" \
    "v_fmac_f32 v204, v176, v176\n\t" \
    "v_fmac_f32 v205, v177, v177\n\t" \
    "v_fmac_f32 v206, v178, v178\n\t" \
    "v_fmac_f32 v207, v179, v179\n\t" \
    "v_fmac_f32 v204, v180, v180\n\t" \
    "v_fmac_f32 v205, v181, v181\n\t" \
    "v_fmac_f32 v206, v182, v182\n\t" \
    "v_fmac_f32 v207, v183, v183\n\t" \
    "s_waitcnt lgkmcnt(0)\n\t" \
    "v_mfma_f32_16x16x32_bf16 v[200:203], v[192:195], v[92:95], v[200:203]\n\t" \
    "v_lshlrev_b32 v184, 16, v92\n\t" \
    "v_and_b32 v185, 0xffff0000, v92\n\t" \
    "v_lshlrev_b32 v186, 16, v93\n\t" \
    "v_and_b32 v187, 0xffff0000, v93\n\t" \
    "v_lshlrev_b32 v188, 16, v94\n\t" \
    "v_and_b32 v189, 0xffff0000, v94\n\t" \
    "v_lshlrev_b32 v190, 16, v95\n\t" \
    "v_and_b32 v191, 0xffff0000, v95\n\t" \
    "v_fmac_f32 v204, v184, v184\n\t" \
    "v_fmac_f32 v205, v185, v185\n\t" \
    "v_fmac_f32 v206, v186, v186\n\t" \
    "v_fmac_f32 v207, v187, v187\n\t" \
    "v_fmac_f32 v204, v188, v188\n\t" \
    "v_fmac_f32 v205, v189, v189\n\t" \
    "v_fmac_f32 v206, v190, v190\n\t" \
    "v_fmac_f32 v207, v191, v191\n\t" \
    "s_barrier\n\t" \
    "ds_read_b128 v[96:99], v212 offset:0\n\t" \
    "ds_read_b128 v[100:103], v213 offset:0\n\t" \
    "ds_read_b128 v[104:107], v214 offset:0\n\t" \
    "ds_read_b128 v[108:111], v215 offset:0\n\t" \
    "s_add_u32 m0, %[lx], 24576\n\t" \
    "s_nop 0\n\t" \
    "global_load_lds_dwordx4 v216, s[40:41]\n\t" \
    "s_waitcnt lgkmcnt(3)\n\t" \
    "v_mfma_f32_16x16x32_bf16 v[200:203], v[192:195], v[96:99], v[200:203]\n\t" \
    "v_lshlrev_b32 v176, 16, v96\n\t" \
    "v_and_b32 v177, 0xffff0000, v96\n\t" \
    "v_lshlrev_b32 v178, 16, v97\n\t" \
    "v_and_b32 v179, 0xffff0000, v97\n\t" \
    "v_lshlrev_b32 v180, 16, v98\n\t" \
    "v_and_b32 v181, 0xffff0000, v98\n\t" \
    "v_lshlrev_b32 v182, 16, v99\n\t" \
    "v_and_b32 v183, 0xffff0000, v99\n\t" \
    "s_add_u32 m0, %[lw], 24576\n\t" \
    "s_nop 0\n\t" \
    "global_load_lds_dwordx4 v220, s[42:43]\n\t" \
    "v_fmac_f32 v204, v176, v176\n\t" \
    "v_fmac_f32 v205, v177, v177\n\t" \
    "v_fmac_f32 v206, v178, v178\n\t" \
    "v_fmac_f32 v207, v179, v179\n\t" \
    "v_fmac_f32 v204, v180, v180\n\t" \
    "v_fmac_f32 v205, v181, v181\n\t" \
    "v_fmac_f32 v206, v182, v182\n\t" \
    "v_fmac_f32 v207, v183, v183\n\t" \
    "s_add_u32 m0, %[lx], 25600\n\t" \
    "s_nop 0\n\t" \
    "global_load_lds_dwordx4 v217, s[40:41]\n\t" \
    "s_waitcnt lgkmcnt(2)\n\t" \
    "v_mfma_f32_16x16x32_bf16 v[200:203], v[192:195], v[100:103], v[200:203]\n\t" \
    "v_lshlrev_b32 v184, 16, v100\n\t" \
    "v_and_b32 v185, 0xffff0000, v100\n\t" \
    "v_lshlrev_b32 v186, 16, v101\n\t" \
    "v_and_b32 v187, 0xffff0000, v101\n\t" \
    "v_lshlrev_b32 v188, 16, v102\n\t" \
    "v_and_b32 v189, 0xffff0000, v102\n\t" \
    "v_lshlrev_b32 v190, 16, v103\n\t" \
    "v_and_b32 v191, 0xffff0000, v103\n\t" \
    "s_add_u32 m0, %[lw], 25600\n\t" \
    "s_nop 0\n\t" \
    "global_load_lds_dwordx4 v221, s[42:43]\n\t" \
    "v_fmac_f32 v204, v184, v184\n\t" \
    "v_fmac_f32 v205, v185, v185\n\t" \
    "v_fmac_f32 v206, v186, v186\n\t" \
    "v_fmac_f32 v207, v187, v187\n\t" \
    "v_fmac_f32 v204, v188, v188\n\t" \
    "v_fmac_f32 v205, v189, v189\n\t" \
    "v_fmac_f32 v206, v190, v190\n\t" \
    "v_fmac_f32 v207, v191, v191\n\t" \
    "s_add_u32 m0, %[lx], 26624\n\t" \
    "s_nop 0\n\t" \
    "global_load_lds_dwordx4 v218, s[40:41]\n\t" \
    "s_waitcnt lgkmcnt(1)\n\t" \
    "v_mfma_f32_16x16x32_bf16 v[200:203], v[192:195], v[104:107], v[200:203]\n\t" \
    "v_lshlrev_b32 v176, 16, v104\n\t" \
    "v_and_b32 v177, 0xffff0000, v104\n\t" \
    "v_lshlrev_b32 v178, 16, v105\n\t" \
    "v_and_b32 v179, 0xffff0000, v105\n\t" \
    "v_lshlrev_b32 v180, 16, v106\n\t" \
    "v_and_b32 v181, 0xffff0000, v106\n\t" \
    "v_lshlrev_b32 v182, 16, v107\n\t" \
    "v_and_b32 v183, 0xffff0000, v107\n\t" \
    "s_add_u32 m0, %[lx], 27648\n\t" \
    "s_nop 0\n\t" \
    "global_load_lds_dwordx4 v219, s[40:41]\n\t" \
    "s_add_u32 s40, s40, 128\n\t" \
    "s_addc_u32 s41, s41, 0\n\t" \
    "s_add_u32 s42, s42, 229376\n\t" \
    "s_addc_u32 s43, s43, 0\n\t" \
    "v_fmac_f32 v204, v176, v176\n\t" \
    "v_fmac_f32 v205, v177, v177\n\t" \
    "v_fmac_f32 v206, v178, v178\n\t" \
    "v_fmac_f32 v207, v179, v179\n\t" \
    "v_fmac_f32 v204, v180, v180\n\t" \
    "v_fmac_f32 v205, v181, v181\n\t" \
    "v_fmac_f32 v206, v182, v182\n\t" \
    "v_fmac_f32 v207, v183, v183\n\t" \
    "s_waitcnt lgkmcnt(0)\n\t" \
    "v_mfma_f32_16x16x32_bf16 v[200:203], v[192:195], v[108:111], v[200:203]\n\t" \
    "v_lshlrev_b32 v184, 16, v108\n\t" \
    "v_and_b32 v185, 0xffff0000, v108\n\t" \
    "v_lshlrev_b32 v186, 16, v109\n\t" \
    "v_and_b32 v187, 0xffff0000, v109\n\t" \
    "v_lshlrev_b32 v188, 16, v110\n\t" \
    "v_and_b32 v189, 0xffff0000, v110\n\t" \
    "v_lshlrev_b32 v190, 16, v111\n\t" \
    "v_and_b32 v191, 0xffff0000, v111\n\t" \
    "v_fmac_f32 v204, v184, v184\n\t" \
    "v_fmac_f32 v205, v185, v185\n\t" \
    "v_fmac_f32 v206, v186, v186\n\t" \
    "v_fmac_f32 v207, v187, v187\n\t" \
    "v_fmac_f32 v204, v188, v188\n\t" \
    "v_fmac_f32 v205, v189, v189\n\t" \
    "v_fmac_f32 v206, v190, v190\n\t" \
    "v_fmac_f32 v207, v191, v191\n\t" \
    "s_waitcnt vmcnt(12)\n\t" \
    "s_barrier\n\t" \
    "ds_read_b128 v[112:115], v212 offset:24576\n\t" \
    "ds_read_b128 v[116:119], v213 offset:24576\n\t" \
    "ds_read_b128 v[120:123], v214 offset:24576\n\t" \
    "ds_read_b128 v[124:127], v215 offset:24576\n\t" \
    "s_add_u32 m0, %[lx], 49152\n\t" \
    "s_nop 0\n\t" \
    "global_load_lds_dwordx4 v216, s[40:41]\n\t" \
    "s_waitcnt lgkmcnt(3)\n\t" \
    "v_mfma_f32_16x16x32_bf16 v[200:203], v[192:195], v[112:115], v[200:203]\n\t" \
    "v_lshlrev_b32 v176, 16, v112\n\t" \
    "v_and_b32 v177, 0xffff0000, v112\n\t" \
    "v_lshlrev_b32 v178, 16, v113\n\t" \
    "v_and_b32 v179, 0xffff0000, v113\n\t" \
    "v_lshlrev_b32 v180, 16, v114\n\t" \
    "v_and_b32 v181, 0xffff0000, v114\n\t" \
    "v_lshlrev_b32 v182, 16, v115\n\t" \
    "v_and_b32 v183, 0xffff0000, v115\n\t" \
    "s_add_u32 m0, %[lw], 49152\n\t" \
    "s_nop 0\n\t" \
    "global_load_lds_dwordx4 v220, s[42:43]\n\t" \
    "v_fmac_f32 v204, v176, v176\n\t" \
    "v_fmac_f32 v205, v177, v177\n\t" \
    "v_fmac_f32 v206, v178, v178\n\t" \
    "v_fmac_f32 v207, v179, v179\n\t" \
    "v_fmac_f32 v204, v180, v180\n\t" \
    "v_fmac_f32 v205, v181, v181\n\t" \
    "v_fmac_f32 v206, v182, v182\n\t" \
    "v_fmac_f32 v207, v183, v183\n\t" \
    "s_add_u32 m0, %[lx], 50176\n\t" \
    "s_nop 0\n\t" \
    "global_load_lds_dwordx4 v217, s[40:41]\n\t" \
    "s_waitcnt lgkmcnt(2)\n\t" \
    "v_mfma_f32_16x16x32_bf16 v[200:203], v[192:195], v[116:119], v[200:203]\n\t" \
    "v_lshlrev_b32 v184, 16, v116\n\t" \
    "v_and_b32 v185, 0xffff0000, v116\n\t" \
    "v_lshlrev_b32 v186, 16, v117\n\t" \
    "v_and_b32 v187, 0xffff0000, v117\n\t" \
    "v_lshlrev_b32 v188, 16, v118\n\t" \
    "v_and_b32 v189, 0xffff0000, v118\n\t" \
    "v_lshlrev_b32 v190, 16, v119\n\t" \
    "v_and_b32 v191, 0xffff0000, v119\n\t" \
    "s_add_u32 m0, %[lw], 50176\n\t" \
    "s_nop 0\n\t" \
    "global_load_lds_dwordx4 v221, s[42:43]\n\t" \
    "v_fmac_f32 v204, v184, v184\n\t" \
    "v_fmac_f32 v205, v185, v185\n\t" \
    "v_fmac_f32 v206, v186, v186\n\t" \
    "v_fmac_f32 v207, v187, v187\n\t" \
    "v_fmac_f32 v204, v188, v188\n\t" \
    "v_fmac_f32 v205, v189, v189\n\t" \
    "v_fmac_f32 v206, v190, v190\n\t" \
    "v_fmac_f32 v207, v191, v191\n\t" \
    "s_add_u32 m0, %[lx], 51200\n\t" \
    "s_nop 0\n\t" \
    "global_load_lds_dwordx4 v218, s[40:41]\n\t" \
    "s_waitcnt lgkmcnt(1)\n\t" \
    "v_mfma_f32_16x16x32_bf16 v[200:203], v[192:195], v[120:123], v[200:203]\n\t" \
    "v_lshlrev_b32 v176, 16, v120\n\t" \
    "v_and_b32 v177, 0xffff0000, v120\n\t" \
    "v_lshlrev_b32 v178, 16, v121\n\t" \
    "v_and_b32 v179, 0xffff0000, v121\n\t" \
    "v_lshlrev_b32 v180, 16, v122\n\t" \
    "v_and_b32 v181, 0xffff0000, v122\n\t" \
    "v_lshlrev_b32 v182, 16, v123\n\t" \
    "v_and_b32 v183, 0xffff0000, v123\n\t" \
    "s_add_u32 m0, %[lx], 52224\n\t" \
    "s_nop 0\n\t" \
    "global_load_lds_dwordx4 v219, s[40:41]\n\t" \
    "s_add_u32 s40, s40, 128\n\t" \
    "s_addc_u32 s41, s41, 0\n\t" \
    "s_add_u32 s42, s42, 229376\n\t" \
    "s_addc_u32 s43, s43, 0\n\t" \
    "v_fmac_f32 v204, v176, v176\n\t" \
    "v_fmac_f32 v205, v177, v177\n\t" \
    "v_fmac_f32 v206, v178, v178\n\t" \
    "v_fmac_f32 v207, v179, v179\n\t" \
    "v_fmac_f32 v204, v180, v180\n\t" \
    "v_fmac_f32 v205, v181, v181\n\t" \
    "v_fmac_f32 v206, v182, v182\n\t" \
    "v_fmac_f32 v207, v183, v183\n\t" \
    "s_waitcnt lgkmcnt(0)\n\t" \
    "v_mfma_f32_16x16x32_bf16 v[200:203], v[192:195], v[124:127], v[200:203]\n\t" \
    "v_lshlrev_b32 v184, 16, v124\n\t" \
    "v_and_b32 v185, 0xffff0000, v124\n\t" \
    "v_lshlrev_b32 v186, 16, v125\n\t" \
    "v_and_b32 v187, 0xffff0000, v125\n\t" \
    "v_lshlrev_b32 v188, 16, v126\n\t" \
    "v_and_b32 v189, 0xffff0000, v126\n\t" \
    "v_lshlrev_b32 v190, 16, v127\n\t" \
    "v_and_b32 v191, 0xffff0000, v127\n\t" \
    "v_fmac_f32 v204, v184, v184\n\t" \
    "v_fmac_f32 v205, v185, v185\n\t" \
    "v_fmac_f32 v206, v186, v186\n\t" \
    "v_fmac_f32 v207, v187, v187\n\t" \
    "v_fmac_f32 v204, v188, v188\n\t" \
    "v_fmac_f32 v205, v189, v189\n\t" \
    "v_fmac_f32 v206, v190, v190\n\t" \
    "v_fmac_f32 v207, v191, v191\n\t" \
    "s_waitcnt vmcnt(12)\n\t" \
    "s_barrier\n\t" \
    "ds_read_b128 v[128:131], v208 offset:0\n\t" \
    "ds_read_b128 v[132:135], v209 offset:0\n\t" \
    "ds_read_b128 v[136:139], v210 offset:0\n\t" \
    "ds_read_b128 v[140:143], v211 offset:0\n\t" \
    "s_add_u32 m0, %[lb], 73728\n\t" \
    "s_nop 0\n\t" \
    "global_load_lds_dwordx4 v224, s[44:45]\n\t" \
    "s_waitcnt lgkmcnt(3)\n\t" \
    "v_mfma_f32_16x16x32_bf16 v[200:203], v[192:195], v[128:131], v[200:203]\n\t" \
    "v_lshlrev_b32 v176, 16, v128\n\t" \
    "v_and_b32 v177, 0xffff0000, v128\n\t" \
    "v_lshlrev_b32 v178, 16, v129\n\t" \
    "v_and_b32 v179, 0xffff0000, v129\n\t" \
    "v_lshlrev_b32 v180, 16, v130\n\t" \
    "v_and_b32 v181, 0xffff0000, v130\n\t" \
    "v_lshlrev_b32 v182, 16, v131\n\t" \
    "v_and_b32 v183, 0xffff0000, v131\n\t" \
    "s_add_u32 m0, %[lb], 74752\n\t" \
    "s_nop 0\n\t" \
    "global_load_lds_dwordx4 v225, s[44:45]\n\t" \
    "v_fmac_f32 v204, v176, v176\n\t" \
    "v_fmac_f32 v205, v177, v177\n\t" \
    "v_fmac_f32 v206, v178, v178\n\t" \
    "v_fmac_f32 v207, v179, v179\n\t" \
    "v_fmac_f32 v204, v180, v180\n\t" \
    "v_fmac_f32 v205, v181, v181\n\t" \
    "v_fmac_f32 v206, v182, v182\n\t" \
    "v_fmac_f32 v207, v183, v183\n\t" \
    "s_add_u32 m0, %[lb], 75776\n\t" \
    "s_nop 0\n\t" \
    "global_load_lds_dwordx4 v226, s[44:45]\n\t" \
    "s_add_u32 s44, s44, 43008\n\t" \
    "s_addc_u32 s45, s45, 0\n\t" \
    "s_waitcnt lgkmcnt(2)\n\t" \
    "v_mfma_f32_16x16x32_bf16 v[200:203], v[192:195], v[132:135], v[200:203]\n\t" \
    "v_lshlrev_b32 v184, 16, v132\n\t" \
    "v_and_b32 v185, 0xffff0000, v132\n\t" \
    "v_lshlrev_b32 v186, 16, v133\n\t" \
    "v_and_b32 v187, 0xffff0000, v133\n\t" \
    "v_lshlrev_b32 v188, 16, v134\n\t" \
    "v_and_b32 v189, 0xffff0000, v134\n\t" \
    "v_lshlrev_b32 v190, 16, v135\n\t" \
    "v_and_b32 v191, 0xffff0000, v135\n\t" \
    "s_add_u32 m0, %[lb], 76800\n\t" \
    "s_nop 0\n\t" \
    "global_load_lds_dwordx4 v224, s[44:45]\n\t" \
    "v_fmac_f32 v204, v184, v184\n\t" \
    "v_fmac_f32 v205, v185, v185\n\t" \
    "v_fmac_f32 v206, v186, v186\n\t" \
    "v_fmac_f32 v207, v187, v187\n\t" \
    "v_fmac_f32 v204, v188, v188\n\t" \
    "v_fmac_f32 v205, v189, v189\n\t" \
    "v_fmac_f32 v206, v190, v190\n\t" \
    "v_fmac_f32 v207, v191, v191\n\t" \
    "s_add_u32 m0, %[lb], 77824\n\t" \
    "s_nop 0\n\t" \
    "global_load_lds_dwordx4 v225, s[44:45]\n\t" \
    "s_waitcnt lgkmcnt(1)\n\t" \
    "v_mfma_f32_16x16x32_bf16 v[200:203], v[192:195], v[136:139], v[200:203]\n\t" \
    "v_lshlrev_b32 v176, 16, v136\n\t" \
    "v_and_b32 v177, 0xffff0000, v136\n\t" \
    "v_lshlrev_b32 v178, 16, v137\n\t" \
    "v_and_b32 v179, 0xffff0000, v137\n\t" \
    "v_lshlrev_b32 v180, 16, v138\n\t" \
    "v_and_b32 v181, 0xffff0000, v138\n\t" \
    "v_lshlrev_b32 v182, 16, v139\n\t" \
    "v_and_b32 v183, 0xffff0000, v139\n\t" \
    "s_add_u32 m0, %[lb], 78848\n\t" \
    "s_nop 0\n\t" \
    "global_load_lds_dwordx4 v226, s[44:45]\n\t" \
    "s_add_u32 s44, s44, 301056\n\t" \
    "s_addc_u32 s45, s45, 0\n\t" \
    "v_fmac_f32 v204, v176, v176\n\t" \
    "v_fmac_f32 v205, v177, v177\n\t" \
    "v_fmac_f32 v206, v178, v178\n\t" \
    "v_fmac_f32 v207, v179, v179\n\t" \
    "v_fmac_f32 v204, v180, v180\n\t" \
    "v_fmac_f32 v205, v181, v181\n\t" \
    "v_fmac_f32 v206, v182, v182\n\t" \
    "v_fmac_f32 v207, v183, v183\n\t" \
    "s_waitcnt lgkmcnt(0)\n\t" \
    "v_mfma_f32_16x16x32_bf16 v[200:203], v[192:195], v[140:143], v[200:203]\n\t" \
    "v_lshlrev_b32 v184, 16, v140\n\t" \
    "v_and_b32 v185, 0xffff0000, v140\n\t" \
    "v_lshlrev_b32 v186, 16, v141\n\t" \
    "v_and_b32 v187, 0xffff0000, v141\n\t" \
    "v_lshlrev_b32 v188, 16, v142\n\t" \
    "v_and_b32 v189, 0xffff0000, v142\n\t" \
    "v_lshlrev_b32 v190, 16, v143\n\t" \
    "v_and_b32 v191, 0xffff0000, v143\n\t" \
    "v_fmac_f32 v204, v184, v184\n\t" \
    "v_fmac_f32 v205, v185, v185\n\t" \
    "v_fmac_f32 v206, v186, v186\n\t" \
    "v_fmac_f32 v207, v187, v187\n\t" \
    "v_fmac_f32 v204, v188, v188\n\t" \
    "v_fmac_f32 v205, v189, v189\n\t" \
    "v_fmac_f32 v206, v190, v190\n\t" \
    "v_fmac_f32 v207, v191, v191\n\t" \
    "s_waitcnt vmcnt(12)\n\t" \
    "s_barrier\n\t" \
    "ds_read_b128 v[144:147], v208 offset:24576\n\t" \
    "ds_read_b128 v[148:151], v209 offset:24576\n\t" \
    "ds_read_b128 v[152:155], v210 offset:24576\n\t" \
    "ds_read_b128 v[156:159], v211 offset:24576\n\t" \
    "s_add_u32 m0, %[lb], 0\n\t" \
    "s_nop 0\n\t" \
    "global_load_lds_dwordx4 v224, s[44:45]\n\t" \
    "s_waitcnt lgkmcnt(3)\n\t" \
    "v_mfma_f32_16x16x32_bf16 v[200:203], v[192:195], v[144:147], v[200:203]\n\t" \
    "v_lshlrev_b32 v176, 16, v144\n\t" \
    "v_and_b32 v177, 0xffff0000, v144\n\t" \
    "v_lshlrev_b32 v178, 16, v145\n\t" \
    "v_and_b32 v179, 0xffff0000, v145\n\t" \
    "v_lshlrev_b32 v180, 16, v146\n\t" \
    "v_and_b32 v181, 0xffff0000, v146\n\t" \
    "v_lshlrev_b32 v182, 16, v147\n\t" \
    "v_and_b32 v183, 0xffff0000, v147\n\t" \
    "s_add_u32 m0, %[lb], 1024\n\t" \
    "s_nop 0\n\t" \
    "global_load_lds_dwordx4 v225, s[44:45]\n\t" \
    "v_fmac_f32 v204, v176, v176\n\t" \
    "v_fmac_f32 v205, v177, v177\n\t" \
    "v_fmac_f32 v206, v178, v178\n\t" \
    "v_fmac_f32 v207, v179, v179\n\t" \
    "v_fmac_f32 v204, v180, v180\n\t" \
    "v_fmac_f32 v205, v181, v181\n\t" \
    "v_fmac_f32 v206, v182, v182\n\t" \
    "v_fmac_f32 v207, v183, v183\n\t" \
    "s_add_u32 m0, %[lb], 2048\n\t" \
    "s_nop 0\n\t" \
    "global_load_lds_dwordx4 v226, s[44:45]\n\t" \
    "s_add_u32 s44, s44, 43008\n\t" \
    "s_addc_u32 s45, s45, 0\n\t" \
    "s_waitcnt lgkmcnt(2)\n\t" \
    "v_mfma_f32_16x16x32_bf16 v[200:203], v[192:195], v[148:151], v[200:203]\n\t" \
    "v_lshlrev_b32 v184, 16, v148\n\t" \
    "v_and_b32 v185, 0xffff0000, v148\n\t" \
    "v_lshlrev_b32 v186, 16, v149\n\t" \
    "v_and_b32 v187, 0xffff0000, v149\n\t" \
    "v_lshlrev_b32 v188, 16, v150\n\t" \
    "v_and_b32 v189, 0xffff0000, v150\n\t" \
    "v_lshlrev_b32 v190, 16, v151\n\t" \
    "v_and_b32 v191, 0xffff0000, v151\n\t" \
    "s_add_u32 m0, %[lb], 3072\n\t" \
    "s_nop 0\n\t" \
    "global_load_lds_dwordx4 v224, s[44:45]\n\t" \
    "v_fmac_f32 v204, v184, v184\n\t" \
    "v_fmac_f32 v205, v185, v185\n\t" \
    "v_fmac_f32 v206, v186, v186\n\t" \
    "v_fmac_f32 v207, v187, v187\n\t" \
    "v_fmac_f32 v204, v188, v188\n\t" \
    "v_fmac_f32 v205, v189, v189\n\t" \
    "v_fmac_f32 v206, v190, v190\n\t" \
    "v_fmac_f32 v207, v191, v191\n\t" \
    "s_add_u32 m0, %[lb], 4096\n\t" \
    "s_nop 0\n\t" \
    "global_load_lds_dwordx4 v225, s[44:45]\n\t" \
    "s_waitcnt lgkmcnt(1)\n\t" \
    "v_mfma_f32_16x16x32_bf16 v[200:203], v[192:195], v[152:155], v[200:203]\n\t" \
    "v_lshlrev_b32 v176, 16, v152\n\t" \
    "v_and_b32 v177, 0xffff0000, v152\n\t" \
    "v_lshlrev_b32 v178, 16, v153\n\t" \
    "v_and_b32 v179, 0xffff0000, v153\n\t" \
    "v_lshlrev_b32 v180, 16, v154\n\t" \
    "v_and_b32 v181, 0xffff0000, v154\n\t" \
    "v_lshlrev_b32 v182, 16, v155\n\t" \
    "v_and_b32 v183, 0xffff0000, v155\n\t" \
    "s_add_u32 m0, %[lb], 5120\n\t" \
    "s_nop 0\n\t" \
    "global_load_lds_dwordx4 v226, s[44:45]\n\t" \
    "s_add_u32 s44, s44, 301056\n\t" \
    "s_addc_u32 s45, s45, 0\n\t" \
    "v_fmac_f32 v204, v176, v176\n\t" \
    "v_fmac_f32 v205, v177, v177\n\t" \
    "v_fmac_f32 v206, v178, v178\n\t" \
    "v_fmac_f32 v207, v179, v179\n\t" \
    "v_fmac_f32 v204, v180, v180\n\t" \
    "v_fmac_f32 v205, v181, v181\n\t" \
    "v_fmac_f32 v206, v182, v182\n\t" \
    "v_fmac_f32 v207, v183, v183\n\t" \
    "s_waitcnt lgkmcnt(0)\n\t" \
    "v_mfma_f32_16x16x32_bf16 v[200:203], v[192:195], v[156:159], v[200:203]\n\t" \
    "v_lshlrev_b32 v184, 16, v156\n\t" \
    "v_and_b32 v185, 0xffff0000, v156\n\t" \
    "v_lshlrev_b32 v186, 16, v157\n\t" \
    "v_and_b32 v187, 0xffff0000, v157\n\t" \
    "v_lshlrev_b32 v188, 16, v158\n\t" \
    "v_and_b32 v189, 0xffff0000, v158\n\t" \
    "v_lshlrev_b32 v190, 16, v159\n\t" \
    "v_and_b32 v191, 0xffff0000, v159\n\t" \
    "v_fmac_f32 v204, v184, v184\n\t" \
    "v_fmac_f32 v205, v185, v185\n\t" \
    "v_fmac_f32 v206, v186, v186\n\t" \
    "v_fmac_f32 v207, v187, v187\n\t" \
    "v_fmac_f32 v204, v188, v188\n\t" \
    "v_fmac_f32 v205, v189, v189\n\t" \
    "v_fmac_f32 v206, v190, v190\n\t" \
    "v_fmac_f32 v207, v191, v191\n\t" \
    "s_waitcnt vmcnt(12)\n\t" \
    "s_barrier\n\t" \
    "ds_read_b128 v[160:163], v212 offset:0\n\t" \
    "ds_read_b128 v[164:167], v213 offset:0\n\t" \
    "ds_read_b128 v[168:171], v214 offset:0\n\t" \
    "ds_read_b128 v[172:175], v215 offset:0\n\t" \
    "s_add_u32 m0, %[lb], 24576\n\t" \
    "s_nop 0\n\t" \
    "global_load_lds_dwordx4 v224, s[44:45]\n\t" \
    "s_waitcnt lgkmcnt(3)\n\t" \
    "v_mfma_f32_16x16x32_bf16 v[200:203], v[192:195], v[160:163], v[200:203]\n\t" \
    "v_lshlrev_b32 v176, 16, v160\n\t" \
    "v_and_b32 v177, 0xffff0000, v160\n\t" \
    "v_lshlrev_b32 v178, 16, v161\n\t" \
    "v_and_b32 v179, 0xffff0000, v161\n\t" \
    "v_lshlrev_b32 v180, 16, v162\n\t" \
    "v_and_b32 v181, 0xffff0000, v162\n\t" \
    "v_lshlrev_b32 v182, 16, v163\n\t" \
    "v_and_b32 v183, 0xffff0000, v163\n\t" \
    "s_add_u32 m0, %[lb], 25600\n\t" \
    "s_nop 0\n\t" \
    "global_load_lds_dwordx4 v225, s[44:45]\n\t" \
    "v_fmac_f32 v204, v176, v176\n\t" \
    "v_fmac_f32 v205, v177, v177\n\t" \
    "v_fmac_f32 v206, v178, v178\n\t" \
    "v_fmac_f32 v207, v179, v179\n\t" \
    "v_fmac_f32 v204, v180, v180\n\t" \
    "v_fmac_f32 v205, v181, v181\n\t" \
    "v_fmac_f32 v206, v182, v182\n\t" \
    "v_fmac_f32 v207, v183, v183\n\t" \
    "s_add_u32 m0, %[lb], 26624\n\t" \
    "s_nop 0\n\t" \
    "global_load_lds_dwordx4 v226, s[44:45]\n\t" \
    "s_add_u32 s44, s44, 43008\n\t" \
    "s_addc_u32 s45, s45, 0\n\t" \
    "s_waitcnt lgkmcnt(2)\n\t" \
    "v_mfma_f32_16x16x32_bf16 v[200:203], v[192:195], v[164:167], v[200:203]\n\t" \
    "v_lshlrev_b32 v184, 16, v164\n\t" \
    "v_and_b32 v185, 0xffff0000, v164\n\t" \
    "v_lshlrev_b32 v186, 16, v165\n\t" \
    "v_and_b32 v187, 0xffff0000, v165\n\t" \
    "v_lshlrev_b32 v188, 16, v166\n\t" \
    "v_and_b32 v189, 0xffff0000, v166\n\t" \
    "v_lshlrev_b32 v190, 16, v167\n\t" \
    "v_and_b32 v191, 0xffff0000, v167\n\t" \
    "s_add_u32 m0, %[lb], 27648\n\t" \
    "s_nop 0\n\t" \
    "global_load_lds_dwordx4 v224, s[44:45]\n\t" \
    "v_fmac_f32 v204, v184, v184\n\t" \
    "v_fmac_f32 v205, v185, v185\n\t" \
    "v_fmac_f32 v206, v186, v186\n\t" \
    "v_fmac_f32 v207, v187, v187\n\t" \
    "v_fmac_f32 v204, v188, v188\n\t" \
    "v_fmac_f32 v205, v189, v189\n\t" \
    "v_fmac_f32 v206, v190, v190\n\t" \
    "v_fmac_f32 v207, v191, v191\n\t" \
    "s_add_u32 m0, %[lb], 28672\n\t" \
    "s_nop 0\n\t" \
    "global_load_lds_dwordx4 v225, s[44:45]\n\t" \
    "s_waitcnt lgkmcnt(1)\n\t" \
    "v_mfma_f32_16x16x32_bf16 v[200:203], v[192:195], v[168:171], v[200:203]\n\t" \
    "v_lshlrev_b32 v176, 16, v168\n\t" \
    "v_and_b32 v177, 0xffff0000, v168\n\t" \
    "v_lshlrev_b32 v178, 16, v169\n\t" \
    "v_and_b32 v179, 0xffff0000, v169\n\t" \
    "v_lshlrev_b32 v180, 16, v170\n\t" \
    "v_and_b32 v181, 0xffff0000, v170\n\t" \
    "v_lshlrev_b32 v182, 16, v171\n\t" \
    "v_and_b32 v183, 0xffff0000, v171\n\t" \
    "s_add_u32 m0, %[lb], 29696\n\t" \
    "s_nop 0\n\t" \
    "global_load_lds_dwordx4 v226, s[44:45]\n\t" \
    "s_add_u32 s44, s44, 301056\n\t" \
    "s_addc_u32 s45, s45, 0\n\t" \
    "v_fmac_f32 v204, v176, v176\n\t" \
    "v_fmac_f32 v205, v177, v177\n\t" \
    "v_fmac_f32 v206, v178, v178\n\t" \
    "v_fmac_f32 v207, v179, v179\n\t" \
    "v_fmac_f32 v204, v180, v180\n\t" \
    "v_fmac_f32 v205, v181, v181\n\t" \
    "v_fmac_f32 v206, v182, v182\n\t" \
    "v_fmac_f32 v207, v183, v183\n\t" \
    "s_waitcnt lgkmcnt(0)\n\t" \
    "v_mfma_f32_16x16x32_bf16 v[200:203], v[192:195], v[172:175], v[200:203]\n\t" \
    "v_lshlrev_b32 v184, 16, v172\n\t" \
    "v_and_b32 v185, 0xffff0000, v172\n\t" \
    "v_lshlrev_b32 v186, 16, v173\n\t" \
    "v_and_b32 v187, 0xffff0000, v173\n\t" \
    "v_lshlrev_b32 v188, 16, v174\n\t" \
    "v_and_b32 v189, 0xffff0000, v174\n\t" \
    "v_lshlrev_b32 v190, 16, v175\n\t" \
    "v_and_b32 v191, 0xffff0000, v175\n\t" \
    "v_fmac_f32 v204, v184, v184\n\t" \
    "v_fmac_f32 v205, v185, v185\n\t" \
    "v_fmac_f32 v206, v186, v186\n\t" \
    "v_fmac_f32 v207, v187, v187\n\t" \
    "v_fmac_f32 v204, v188, v188\n\t" \
    "v_fmac_f32 v205, v189, v189\n\t" \
    "v_fmac_f32 v206, v190, v190\n\t" \
    "v_fmac_f32 v207, v191, v191\n\t" \
    "v_add_f32 v204, v204, v205\n\t" \
    "v_add_f32 v206, v206, v207\n\t" \
    "v_add_f32 v204, v204, v206\n\t"
#define K1B1_CLOB "v176", "v177", "v178", "v179", "v180", "v181", "v182", "v183", "v184", "v185", "v186", "v187", "v188", "v189", "v190", "v191", "v201", "v202", "v203", "v205", "v206", "v207"
#define K1B2_ASM \
    "v_mov_b32 v220, 1\n\t" \
    "s_waitcnt vmcnt(12)\n\t" \
    "s_waitcnt lgkmcnt(0)\n\t" \
    "s_barrier\n\t" \
    "ds_read_b64_tr_b16 v[176:177], v232 offset:24576\n\t" \
    "ds_read_b64_tr_b16 v[178:179], v232 offset:25344\n\t" \
    "ds_read_b64_tr_b16 v[180:181], v232 offset:24640\n\t" \
    "ds_read_b64_tr_b16 v[182:183], v232 offset:25408\n\t" \
    "ds_read_b64_tr_b16 v[184:185], v232 offset:24704\n\t" \
    "ds_read_b64_tr_b16 v[186:187], v232 offset:25472\n\t" \
    "s_waitcnt lgkmcnt(4)\n\t" \
    "v_mfma_f32_32x32x16_bf16 v[0:15], v[176:179], v[64:67], 0\n\t" \
    "ds_read_b64_tr_b16 v[188:189], v233 offset:24576\n\t" \
    "ds_read_b64_tr_b16 v[190:191], v233 offset:25344\n\t" \
    "v_mfma_f32_16x16x32_bf16 v[208:211], v[200:203], v[176:179], 0\n\t" \
    "s_sub_u32 s44, s44, %[rgo]\n\t" \
    "s_subb_u32 s45, s45, 0\n\t" \
    "s_add_u32 m0, %[lb3], 49152\n\t" \
    "s_nop 0\n\t" \
    "global_load_lds_dwordx4 v224, s[44:45]\n\t" \
    "s_waitcnt lgkmcnt(4)\n\t" \
    "v_mfma_f32_32x32x16_bf16 v[16:31], v[180:183], v[64:67], 0\n\t" \
    "ds_read_b64_tr_b16 v[192:193], v233 offset:24640\n\t" \
    "ds_read_b64_tr_b16 v[194:195], v233 offset:25408\n\t" \
    "v_mfma_f32_16x16x32_bf16 v[212:215], v[200:203], v[180:183], 0\n\t" \
    "s_add_u32 m0, %[lb3], 50176\n\t" \
    "s_nop 0\n\t" \
    "global_load_lds_dwordx4 v225, s[44:45]\n\t" \
    "s_waitcnt lgkmcnt(4)\n\t" \
    "v_mfma_f32_32x32x16_bf16 v[32:47], v[184:187], v[64:67], 0\n\t" \
    "ds_read_b64_tr_b16 v[196:197], v233 offset:24704\n\t" \
    "ds_read_b64_tr_b16 v[198:199], v233 offset:25472\n\t" \
    "v_mfma_f32_16x16x32_bf16 v[216:219], v[200:203], v[184:187], 0\n\t" \
    "s_waitcnt lgkmcnt(4)\n\t" \
    "v_mfma_f32_32x32x16_bf16 v[0:15], v[188:191], v[68:71], v[0:15]\n\t" \
    "ds_read_b64_tr_b16 v[176:177], v234 offset:24576\n\t" \
    "ds_read_b64_tr_b16 v[178:179], v234 offset:25344\n\t" \
    "s_add_u32 m0, %[lb3], 51200\n\t" \
    "s_nop 0\n\t" \
    "global_load_lds_dwordx4 v226, s[44:45]\n\t" \
    "s_waitcnt lgkmcnt(4)\n\t" \
    "v_mfma_f32_32x32x16_bf16 v[16:31], v[192:195], v[68:71], v[16:31]\n\t" \
    "ds_read_b64_tr_b16 v[180:181], v234 offset:24640\n\t" \
    "ds_read_b64_tr_b16 v[182:183], v234 offset:25408\n\t" \
    "s_waitcnt lgkmcnt(4)\n\t" \
    "v_mfma_f32_32x32x16_bf16 v[32:47], v[196:199], v[68:71], v[32:47]\n\t" \
    "ds_read_b64_tr_b16 v[184:185], v234 offset:24704\n\t" \
    "ds_read_b64_tr_b16 v[186:187], v234 offset:25472\n\t" \
    "s_waitcnt lgkmcnt(4)\n\t" \
    "v_mfma_f32_32x32x16_bf16 v[0:15], v[176:179], v[72:75], v[0:15]\n\t" \
    "ds_read_b64_tr_b16 v[188:189], v235 offset:24576\n\t" \
    "ds_read_b64_tr_b16 v[190:191], v235 offset:25344\n\t" \
    "s_waitcnt lgkmcnt(4)\n\t" \
    "v_mfma_f32_32x32x16_bf16 v[16:31], v[180:183], v[72:75], v[16:31]\n\t" \
    "ds_read_b64_tr_b16 v[192:193], v235 offset:24640\n\t" \
    "ds_read_b64_tr_b16 v[194:195], v235 offset:25408\n\t" \
    "s_waitcnt lgkmcnt(4)\n\t" \
    "v_mfma_f32_32x32x16_bf16 v[32:47], v[184:187], v[72:75], v[32:47]\n\t" \
    "ds_read_b64_tr_b16 v[196:197], v235 offset:24704\n\t" \
    "ds_read_b64_tr_b16 v[198:199], v235 offset:25472\n\t" \
    "s_waitcnt lgkmcnt(4)\n\t" \
    "v_mfma_f32_32x32x16_bf16 v[0:15], v[188:191], v[76:79], v[0:15]\n\t" \
    "ds_read_b64_tr_b16 v[176:177], v232 offset:36864\n\t" \
    "ds_read_b64_tr_b16 v[178:179], v232 offset:37632\n\t" \
    "s_waitcnt lgkmcnt(4)\n\t" \
    "v_mfma_f32_32x32x16_bf16 v[16:31], v[192:195], v[76:79], v[16:31]\n\t" \
    "ds_read_b64_tr_b16 v[180:181], v232 offset:36928\n\t" \
    "ds_read_b64_tr_b16 v[182:183], v232 offset:37696\n\t" \
    "s_waitcnt lgkmcnt(4)\n\t" \
    "v_mfma_f32_32x32x16_bf16 v[32:47], v[196:199], v[76:79], v[32:47]\n\t" \
    "ds_read_b64_tr_b16 v[184:185], v232 offset:36992\n\t" \
    "ds_read_b64_tr_b16 v[186:187], v232 offset:37760\n\t" \
    "s_waitcnt lgkmcnt(4)\n\t" \
    "v_mfma_f32_32x32x16_bf16 v[0:15], v[176:179], v[80:83], v[0:15]\n\t" \
    "ds_read_b64_tr_b16 v[188:189], v233 offset:36864\n\t" \
    "ds_read_b64_tr_b16 v[190:191], v233 offset:37632\n\t" \
    "v_mfma_f32_16x16x32_bf16 v[208:211], v[200:203], v[176:179], v[208:211]\n\t" \
    "s_waitcnt lgkmcnt(4)\n\t" \
    "v_mfma_f32_32x32x16_bf16 v[16:31], v[180:183], v[80:83], v[16:31]\n\t" \
    "ds_read_b64_tr_b16 v[192:193], v233 offset:36928\n\t" \
    "ds_read_b64_tr_b16 v[194:195], v233 offset:37696\n\t" \
    "v_mfma_f32_16x16x32_bf16 v[212:215], v[200:203], v[180:183], v[212:215]\n\t" \
    "s_waitcnt lgkmcnt(4)\n\t" \
    "v_mfma_f32_32x32x16_bf16 v[32:47], v[184:187], v[80:83], v[32:47]\n\t" \
    "ds_read_b64_tr_b16 v[196:197], v233 offset:36992\n\t" \
    "ds_read_b64_tr_b16 v[198:199], v233 offset:37760\n\t" \
    "v_mfma_f32_16x16x32_bf16 v[216:219], v[200:203], v[184:187], v[216:219]\n\t" \
    "s_waitcnt lgkmcnt(4)\n\t" \
    "v_mfma_f32_32x32x16_bf16 v[0:15], v[188:191], v[84:87], v[0:15]\n\t" \
    "ds_read_b64_tr_b16 v[176:177], v234 offset:36864\n\t" \
    "ds_read_b64_tr_b16 v[178:179], v234 offset:37632\n\t" \
    "s_waitcnt lgkmcnt(4)\n\t" \
    "v_mfma_f32_32x32x16_bf16 v[16:31], v[192:195], v[84:87], v[16:31]\n\t" \
    "ds_read_b64_tr_b16 v[180:181], v234 offset:36928\n\t" \
    "ds_read_b64_tr_b16 v[182:183], v234 offset:37696\n\t" \
    "s_waitcnt lgkmcnt(4)\n\t" \
    "v_mfma_f32_32x32x16_bf16 v[32:47], v[196:199], v[84:87], v[32:47]\n\t" \
    "ds_read_b64_tr_b16 v[184:185], v234 offset:36992\n\t" \
    "ds_read_b64_tr_b16 v[186:187], v234 offset:37760\n\t" \
    "s_waitcnt vmcnt(9)\n\t" \
    "s_mov_b64 exec, 1\n\t" \
    "ds_add_u32 v205, v220 offset:0\n\t" \
    "s_mov_b64 exec, -1\n\t" \
    "s_waitcnt lgkmcnt(4)\n\t" \
    "v_mfma_f32_32x32x16_bf16 v[0:15], v[176:179], v[88:91], v[0:15]\n\t" \
    "ds_read_b64_tr_b16 v[188:189], v235 offset:36864\n\t" \
    "ds_read_b64_tr_b16 v[190:191], v235 offset:37632\n\t" \
    "s_waitcnt lgkmcnt(4)\n\t" \
    "v_mfma_f32_32x32x16_bf16 v[16:31], v[180:183], v[88:91], v[16:31]\n\t" \
    "ds_read_b64_tr_b16 v[192:193], v235 offset:36928\n\t" \
    "ds_read_b64_tr_b16 v[194:195], v235 offset:37696\n\t" \
    "s_waitcnt lgkmcnt(4)\n\t" \
    "v_mfma_f32_32x32x16_bf16 v[32:47], v[184:187], v[88:91], v[32:47]\n\t" \
    "ds_read_b64_tr_b16 v[196:197], v235 offset:36992\n\t" \
    "ds_read_b64_tr_b16 v[198:199], v235 offset:37760\n\t" \
    "s_waitcnt lgkmcnt(4)\n\t" \
    "v_mfma_f32_32x32x16_bf16 v[0:15], v[188:191], v[92:95], v[0:15]\n\t" \
    "s_waitcnt lgkmcnt(2)\n\t" \
    "v_mfma_f32_32x32x16_bf16 v[16:31], v[192:195], v[92:95], v[16:31]\n\t" \
    "11:\n\t" \
    "ds_read_b32 v204, v205 offset:0\n\t" \
    "s_waitcnt lgkmcnt(0)\n\t" \
    "v_readfirstlane_b32 %[tmp], v204\n\t" \
    "s_nop 1\n\t" \
    "s_cmp_lt_u32 %[tmp], 4\n\t" \
    "s_cbranch_scc1 11b\n\t" \
    "ds_read_b64_tr_b16 v[176:177], v228 offset:0\n\t" \
    "ds_read_b64_tr_b16 v[178:179], v228 offset:768\n\t" \
    "ds_read_b64_tr_b16 v[180:181], v228 offset:64\n\t" \
    "ds_read_b64_tr_b16 v[182:183], v228 offset:832\n\t" \
    "v_mfma_f32_32x32x16_bf16 v[32:47], v[196:199], v[92:95], v[32:47]\n\t" \
    "ds_read_b64_tr_b16 v[184:185], v228 offset:128\n\t" \
    "ds_read_b64_tr_b16 v[186:187], v228 offset:896\n\t" \
    "s_waitcnt lgkmcnt(4)\n\t" \
    "v_mfma_f32_32x32x16_bf16 v[0:15], v[176:179], v[96:99], v[0:15]\n\t" \
    "ds_read_b64_tr_b16 v[188:189], v229 offset:0\n\t" \
    "ds_read_b64_tr_b16 v[190:191], v229 offset:768\n\t" \
    "v_mfma_f32_16x16x32_bf16 v[208:211], v[200:203], v[176:179], v[208:211]\n\t" \
    "s_waitcnt lgkmcnt(4)\n\t" \
    "v_mfma_f32_32x32x16_bf16 v[16:31], v[180:183], v[96:99], v[16:31]\n\t" \
    "ds_read_b64_tr_b16 v[192:193], v229 offset:64\n\t" \
    "ds_read_b64_tr_b16 v[194:195], v229 offset:832\n\t" \
    "v_mfma_f32_16x16x32_bf16 v[212:215], v[200:203], v[180:183], v[212:215]\n\t" \
    "s_waitcnt lgkmcnt(4)\n\t" \
    "v_mfma_f32_32x32x16_bf16 v[32:47], v[184:187], v[96:99], v[32:47]\n\t" \
    "ds_read_b64_tr_b16 v[196:197], v229 offset:128\n\t" \
    "ds_read_b64_tr_b16 v[198:199], v229 offset:896\n\t" \
    "v_mfma_f32_16x16x32_bf16 v[216:219], v[200:203], v[184:187], v[216:219]\n\t" \
    "s_waitcnt lgkmcnt(4)\n\t" \
    "v_mfma_f32_32x32x16_bf16 v[0:15], v[188:191], v[100:103], v[0:15]\n\t" \
    "ds_read_b64_tr_b16 v[176:177], v230 offset:0\n\t" \
    "ds_read_b64_tr_b16 v[178:179], v230 offset:768\n\t" \
    "s_waitcnt lgkmcnt(4)\n\t" \
    "v_mfma_f32_32x32x16_bf16 v[16:31], v[192:195], v[100:103], v[16:31]\n\t" \
    "ds_read_b64_tr_b16 v[180:181], v230 offset:64\n\t" \
    "ds_read_b64_tr_b16 v[182:183], v230 offset:832\n\t" \
    "s_waitcnt lgkmcnt(4)\n\t" \
    "v_mfma_f32_32x32x16_bf16 v[32:47], v[196:199], v[100:103], v[32:47]\n\t" \
    "ds_read_b64_tr_b16 v[184:185], v230 offset:128\n\t" \
    "ds_read_b64_tr_b16 v[186:187], v230 offset:896\n\t" \
    "s_waitcnt lgkmcnt(4)\n\t" \
    "v_mfma_f32_32x32x16_bf16 v[0:15], v[176:179], v[104:107], v[0:15]\n\t" \
    "ds_read_b64_tr_b16 v[188:189], v231 offset:0\n\t" \
    "ds_read_b64_tr_b16 v[190:191], v231 offset:768\n\t" \
    "s_waitcnt lgkmcnt(4)\n\t" \
    "v_mfma_f32_32x32x16_bf16 v[16:31], v[180:183], v[104:107], v[16:31]\n\t" \
    "ds_read_b64_tr_b16 v[192:193], v231 offset:64\n\t" \
    "ds_read_b64_tr_b16 v[194:195], v231 offset:832\n\t" \
    "s_waitcnt lgkmcnt(4)\n\t" \
    "v_mfma_f32_32x32x16_bf16 v[32:47], v[184:187], v[104:107], v[32:47]\n\t" \
    "ds_read_b64_tr_b16 v[196:197], v231 offset:128\n\t" \
    "ds_read_b64_tr_b16 v[198:199], v231 offset:896\n\t" \
    "s_waitcnt lgkmcnt(4)\n\t" \
    "v_mfma_f32_32x32x16_bf16 v[0:15], v[188:191], v[108:111], v[0:15]\n\t" \
    "ds_read_b64_tr_b16 v[176:177], v228 offset:12288\n\t" \
    "ds_read_b64_tr_b16 v[178:179], v228 offset:13056\n\t" \
    "s_waitcnt lgkmcnt(4)\n\t" \
    "v_mfma_f32_32x32x16_bf16 v[16:31], v[192:195], v[108:111], v[16:31]\n\t" \
    "ds_read_b64_tr_b16 v[180:181], v228 offset:12352\n\t" \
    "ds_read_b64_tr_b16 v[182:183], v228 offset:13120\n\t" \
    "s_waitcnt lgkmcnt(4)\n\t" \
    "v_mfma_f32_32x32x16_bf16 v[32:47], v[196:199], v[108:111], v[32:47]\n\t" \
    "ds_read_b64_tr_b16 v[184:185], v228 offset:12416\n\t" \
    "ds_read_b64_tr_b16 v[186:187], v228 offset:13184\n\t" \
    "s_waitcnt lgkmcnt(4)\n\t" \
    "v_mfma_f32_32x32x16_bf16 v[0:15], v[176:179], v[112:115], v[0:15]\n\t" \
    "ds_read_b64_tr_b16 v[188:189], v229 offset:12288\n\t" \
    "ds_read_b64_tr_b16 v[190:191], v229 offset:13056\n\t" \
    "v_mfma_f32_16x16x32_bf16 v[208:211], v[200:203], v[176:179], v[208:211]\n\t" \
    "s_waitcnt lgkmcnt(4)\n\t" \
    "v_mfma_f32_32x32x16_bf16 v[16:31], v[180:183], v[112:115], v[16:31]\n\t" \
    "ds_read_b64_tr_b16 v[192:193], v229 offset:12352\n\t" \
    "ds_read_b64_tr_b16 v[194:195], v229 offset:13120\n\t" \
    "v_mfma_f32_16x16x32_bf16 v[212:215], v[200:203], v[180:183], v[212:215]\n\t" \
    "s_waitcnt lgkmcnt(4)\n\t" \
    "v_mfma_f32_32x32x16_bf16 v[32:47], v[184:187], v[112:115], v[32:47]\n\t" \
    "ds_read_b64_tr_b16 v[196:197], v229 offset:12416\n\t" \
    "ds_read_b64_tr_b16 v[198:199], v229 offset:13184\n\t" \
    "v_mfma_f32_16x16x32_bf16 v[216:219], v[200:203], v[184:187], v[216:219]\n\t" \
    "s_waitcnt lgkmcnt(4)\n\t" \
    "v_mfma_f32_32x32x16_bf16 v[0:15], v[188:191], v[116:119], v[0:15]\n\t" \
    "ds_read_b64_tr_b16 v[176:177], v230 offset:12288\n\t" \
    "ds_read_b64_tr_b16 v[178:179], v230 offset:13056\n\t" \
    "s_waitcnt lgkmcnt(4)\n\t" \
    "v_mfma_f32_32x32x16_bf16 v[16:31], v[192:195], v[116:119], v[16:31]\n\t" \
    "ds_read_b64_tr_b16 v[180:181], v230 offset:12352\n\t" \
    "ds_read_b64_tr_b16 v[182:183], v230 offset:13120\n\t" \
    "s_waitcnt lgkmcnt(4)\n\t" \
    "v_mfma_f32_32x32x16_bf16 v[32:47], v[196:199], v[116:119], v[32:47]\n\t" \
    "ds_read_b64_tr_b16 v[184:185], v230 offset:12416\n\t" \
    "ds_read_b64_tr_b16 v[186:187], v230 offset:13184\n\t" \
    "s_waitcnt vmcnt(3)\n\t" \
    "s_mov_b64 exec, 1\n\t" \
    "ds_add_u32 v205, v220 offset:0\n\t" \
    "s_mov_b64 exec, -1\n\t" \
    "s_waitcnt lgkmcnt(4)\n\t" \
    "v_mfma_f32_32x32x16_bf16 v[0:15], v[176:179], v[120:123], v[0:15]\n\t" \
    "ds_read_b64_tr_b16 v[188:189], v231 offset:12288\n\t" \
    "ds_read_b64_tr_b16 v[190:191], v231 offset:13056\n\t" \
    "s_waitcnt lgkmcnt(4)\n\t" \
    "v_mfma_f32_32x32x16_bf16 v[16:31], v[180:183], v[120:123], v[16:31]\n\t" \
    "ds_read_b64_tr_b16 v[192:193], v231 offset:12352\n\t" \
    "ds_read_b64_tr_b16 v[194:195], v231 offset:13120\n\t" \
    "s_waitcnt lgkmcnt(4)\n\t" \
    "v_mfma_f32_32x32x16_bf16 v[32:47], v[184:187], v[120:123], v[32:47]\n\t" \
    "ds_read_b64_tr_b16 v[196:197], v231 offset:12416\n\t" \
    "ds_read_b64_tr_b16 v[198:199], v231 offset:13184\n\t" \
    "s_waitcnt lgkmcnt(4)\n\t" \
    "v_mfma_f32_32x32x16_bf16 v[0:15], v[188:191], v[124:127], v[0:15]\n\t" \
    "s_waitcnt lgkmcnt(2)\n\t" \
    "v_mfma_f32_32x32x16_bf16 v[16:31], v[192:195], v[124:127], v[16:31]\n\t" \
    "12:\n\t" \
    "ds_read_b32 v204, v205 offset:0\n\t" \
    "s_waitcnt lgkmcnt(0)\n\t" \
    "v_readfirstlane_b32 %[tmp], v204\n\t" \
    "s_nop 1\n\t" \
    "s_cmp_lt_u32 %[tmp], 8\n\t" \
    "s_cbranch_scc1 12b\n\t" \
    "ds_read_b64_tr_b16 v[176:177], v228 offset:24576\n\t" \
    "ds_read_b64_tr_b16 v[178:179], v228 offset:25344\n\t" \
    "ds_read_b64_tr_b16 v[180:181], v228 offset:24640\n\t" \
    "ds_read_b64_tr_b16 v[182:183], v228 offset:25408\n\t" \
    "v_mfma_f32_32x32x16_bf16 v[32:47], v[196:199], v[124:127], v[32:47]\n\t" \
    "ds_read_b64_tr_b16 v[184:185], v228 offset:24704\n\t" \
    "ds_read_b64_tr_b16 v[186:187], v228 offset:25472\n\t" \
    "s_waitcnt lgkmcnt(4)\n\t" \
    "v_mfma_f32_32x32x16_bf16 v[0:15], v[176:179], v[128:131], v[0:15]\n\t" \
    "ds_read_b64_tr_b16 v[188:189], v229 offset:24576\n\t" \
    "ds_read_b64_tr_b16 v[190:191], v229 offset:25344\n\t" \
    "v_mfma_f32_16x16x32_bf16 v[208:211], v[200:203], v[176:179], v[208:211]\n\t" \
    "s_waitcnt lgkmcnt(4)\n\t" \
    "v_mfma_f32_32x32x16_bf16 v[16:31], v[180:183], v[128:131], v[16:31]\n\t" \
    "ds_read_b64_tr_b16 v[192:193], v229 offset:24640\n\t" \
    "ds_read_b64_tr_b16 v[194:195], v229 offset:25408\n\t" \
    "v_mfma_f32_16x16x32_bf16 v[212:215], v[200:203], v[180:183], v[212:215]\n\t" \
    "s_waitcnt lgkmcnt(4)\n\t" \
    "v_mfma_f32_32x32x16_bf16 v[32:47], v[184:187], v[128:131], v[32:47]\n\t" \
    "ds_read_b64_tr_b16 v[196:197], v229 offset:24704\n\t" \
    "ds_read_b64_tr_b16 v[198:199], v229 offset:25472\n\t" \
    "v_mfma_f32_16x16x32_bf16 v[216:219], v[200:203], v[184:187], v[216:219]\n\t" \
    "s_waitcnt lgkmcnt(4)\n\t" \
    "v_mfma_f32_32x32x16_bf16 v[0:15], v[188:191], v[132:135], v[0:15]\n\t" \
    "ds_read_b64_tr_b16 v[176:177], v230 offset:24576\n\t" \
    "ds_read_b64_tr_b16 v[178:179], v230 offset:25344\n\t" \
    "s_waitcnt lgkmcnt(4)\n\t" \
    "v_mfma_f32_32x32x16_bf16 v[16:31], v[192:195], v[132:135], v[16:31]\n\t" \
    "ds_read_b64_tr_b16 v[180:181], v230 offset:24640\n\t" \
    "ds_read_b64_tr_b16 v[182:183], v230 offset:25408\n\t" \
    "s_waitcnt lgkmcnt(4)\n\t" \
    "v_mfma_f32_32x32x16_bf16 v[32:47], v[196:199], v[132:135], v[32:47]\n\t" \
    "ds_read_b64_tr_b16 v[184:185], v230 offset:24704\n\t" \
    "ds_read_b64_tr_b16 v[186:187], v230 offset:25472\n\t" \
    "s_waitcnt lgkmcnt(4)\n\t" \
    "v_mfma_f32_32x32x16_bf16 v[0:15], v[176:179], v[136:139], v[0:15]\n\t" \
    "ds_read_b64_tr_b16 v[188:189], v231 offset:24576\n\t" \
    "ds_read_b64_tr_b16 v[190:191], v231 offset:25344\n\t" \
    "s_waitcnt lgkmcnt(4)\n\t" \
    "v_mfma_f32_32x32x16_bf16 v[16:31], v[180:183], v[136:139], v[16:31]\n\t" \
    "ds_read_b64_tr_b16 v[192:193], v231 offset:24640\n\t" \
    "ds_read_b64_tr_b16 v[194:195], v231 offset:25408\n\t" \
    "s_waitcnt lgkmcnt(4)\n\t" \
    "v_mfma_f32_32x32x16_bf16 v[32:47], v[184:187], v[136:139], v[32:47]\n\t" \
    "ds_read_b64_tr_b16 v[196:197], v231 offset:24704\n\t" \
    "ds_read_b64_tr_b16 v[198:199], v231 offset:25472\n\t" \
    "s_waitcnt lgkmcnt(4)\n\t" \
    "v_mfma_f32_32x32x16_bf16 v[0:15], v[188:191], v[140:143], v[0:15]\n\t" \
    "ds_read_b64_tr_b16 v[176:177], v228 offset:36864\n\t" \
    "ds_read_b64_tr_b16 v[178:179], v228 offset:37632\n\t" \
    "s_waitcnt lgkmcnt(4)\n\t" \
    "v_mfma_f32_32x32x16_bf16 v[16:31], v[192:195], v[140:143], v[16:31]\n\t" \
    "ds_read_b64_tr_b16 v[180:181], v228 offset:36928\n\t" \
    "ds_read_b64_tr_b16 v[182:183], v228 offset:37696\n\t" \
    "s_waitcnt lgkmcnt(4)\n\t" \
    "v_mfma_f32_32x32x16_bf16 v[32:47], v[196:199], v[140:143], v[32:47]\n\t" \
    "ds_read_b64_tr_b16 v[184:185], v228 offset:36992\n\t" \
    "ds_read_b64_tr_b16 v[186:187], v228 offset:37760\n\t" \
    "s_waitcnt lgkmcnt(4)\n\t" \
    "v_mfma_f32_32x32x16_bf16 v[0:15], v[176:179], v[144:147], v[0:15]\n\t" \
    "ds_read_b64_tr_b16 v[188:189], v229 offset:36864\n\t" \
    "ds_read_b64_tr_b16 v[190:191], v229 offset:37632\n\t" \
    "v_mfma_f32_16x16x32_bf16 v[208:211], v[200:203], v[176:179], v[208:211]\n\t" \
    "s_waitcnt lgkmcnt(4)\n\t" \
    "v_mfma_f32_32x32x16_bf16 v[16:31], v[180:183], v[144:147], v[16:31]\n\t" \
    "ds_read_b64_tr_b16 v[192:193], v229 offset:36928\n\t" \
    "ds_read_b64_tr_b16 v[194:195], v229 offset:37696\n\t" \
    "v_mfma_f32_16x16x32_bf16 v[212:215], v[200:203], v[180:183], v[212:215]\n\t" \
    "s_waitcnt lgkmcnt(4)\n\t" \
    "v_mfma_f32_32x32x16_bf16 v[32:47], v[184:187], v[144:147], v[32:47]\n\t" \
    "ds_read_b64_tr_b16 v[196:197], v229 offset:36992\n\t" \
    "ds_read_b64_tr_b16 v[198:199], v229 offset:37760\n\t" \
    "v_mfma_f32_16x16x32_bf16 v[216:219], v[200:203], v[184:187], v[216:219]\n\t" \
    "s_waitcnt lgkmcnt(4)\n\t" \
    "v_mfma_f32_32x32x16_bf16 v[0:15], v[188:191], v[148:151], v[0:15]\n\t" \
    "ds_read_b64_tr_b16 v[176:177], v230 offset:36864\n\t" \
    "ds_read_b64_tr_b16 v[178:179], v230 offset:37632\n\t" \
    "s_waitcnt lgkmcnt(4)\n\t" \
    "v_mfma_f32_32x32x16_bf16 v[16:31], v[192:195], v[148:151], v[16:31]\n\t" \
    "ds_read_b64_tr_b16 v[180:181], v230 offset:36928\n\t" \
    "ds_read_b64_tr_b16 v[182:183], v230 offset:37696\n\t" \
    "s_waitcnt lgkmcnt(4)\n\t" \
    "v_mfma_f32_32x32x16_bf16 v[32:47], v[196:199], v[148:151], v[32:47]\n\t" \
    "ds_read_b64_tr_b16 v[184:185], v230 offset:36992\n\t" \
    "ds_read_b64_tr_b16 v[186:187], v230 offset:37760\n\t" \
    "s_waitcnt vmcnt(0)\n\t" \
    "s_mov_b64 exec, 1\n\t" \
    "ds_add_u32 v205, v220 offset:0\n\t" \
    "s_mov_b64 exec, -1\n\t" \
    "s_waitcnt lgkmcnt(4)\n\t" \
    "v_mfma_f32_32x32x16_bf16 v[0:15], v[176:179], v[152:155], v[0:15]\n\t" \
    "ds_read_b64_tr_b16 v[188:189], v231 offset:36864\n\t" \
    "ds_read_b64_tr_b16 v[190:191], v231 offset:37632\n\t" \
    "s_waitcnt lgkmcnt(4)\n\t" \
    "v_mfma_f32_32x32x16_bf16 v[16:31], v[180:183], v[152:155], v[16:31]\n\t" \
    "ds_read_b64_tr_b16 v[192:193], v231 offset:36928\n\t" \
    "ds_read_b64_tr_b16 v[194:195], v231 offset:37696\n\t" \
    "s_waitcnt lgkmcnt(4)\n\t" \
    "v_mfma_f32_32x32x16_bf16 v[32:47], v[184:187], v[152:155], v[32:47]\n\t" \
    "ds_read_b64_tr_b16 v[196:197], v231 offset:36992\n\t" \
    "ds_read_b64_tr_b16 v[198:199], v231 offset:37760\n\t" \
    "s_waitcnt lgkmcnt(4)\n\t" \
    "v_mfma_f32_32x32x16_bf16 v[0:15], v[188:191], v[156:159], v[0:15]\n\t" \
    "s_waitcnt lgkmcnt(2)\n\t" \
    "v_mfma_f32_32x32x16_bf16 v[16:31], v[192:195], v[156:159], v[16:31]\n\t" \
    "13:\n\t" \
    "ds_read_b32 v204, v205 offset:0\n\t" \
    "s_waitcnt lgkmcnt(0)\n\t" \
    "v_readfirstlane_b32 %[tmp], v204\n\t" \
    "s_nop 1\n\t" \
    "s_cmp_lt_u32 %[tmp], 12\n\t" \
    "s_cbranch_scc1 13b\n\t" \
    "ds_read_b64_tr_b16 v[176:177], v232 offset:0\n\t" \
    "ds_read_b64_tr_b16 v[178:179], v232 offset:768\n\t" \
    "ds_read_b64_tr_b16 v[180:181], v232 offset:64\n\t" \
    "ds_read_b64_tr_b16 v[182:183], v232 offset:832\n\t" \
    "v_mfma_f32_32x32x16_bf16 v[32:47], v[196:199], v[156:159], v[32:47]\n\t" \
    "ds_read_b64_tr_b16 v[184:185], v232 offset:128\n\t" \
    "ds_read_b64_tr_b16 v[186:187], v232 offset:896\n\t" \
    "s_waitcnt lgkmcnt(4)\n\t" \
    "v_mfma_f32_32x32x16_bf16 v[0:15], v[176:179], v[160:163], v[0:15]\n\t" \
    "ds_read_b64_tr_b16 v[188:189], v233 offset:0\n\t" \
    "ds_read_b64_tr_b16 v[190:191], v233 offset:768\n\t" \
    "v_mfma_f32_16x16x32_bf16 v[208:211], v[200:203], v[176:179], v[208:211]\n\t" \
    "s_waitcnt lgkmcnt(4)\n\t" \
    "v_mfma_f32_32x32x16_bf16 v[16:31], v[180:183], v[160:163], v[16:31]\n\t" \
    "ds_read_b64_tr_b16 v[192:193], v233 offset:64\n\t" \
    "ds_read_b64_tr_b16 v[194:195], v233 offset:832\n\t" \
    "v_mfma_f32_16x16x32_bf16 v[212:215], v[200:203], v[180:183], v[212:215]\n\t" \
    "s_waitcnt lgkmcnt(4)\n\t" \
    "v_mfma_f32_32x32x16_bf16 v[32:47], v[184:187], v[160:163], v[32:47]\n\t" \
    "ds_read_b64_tr_b16 v[196:197], v233 offset:128\n\t" \
    "ds_read_b64_tr_b16 v[198:199], v233 offset:896\n\t" \
    "v_mfma_f32_16x16x32_bf16 v[216:219], v[200:203], v[184:187], v[216:219]\n\t" \
    "s_waitcnt lgkmcnt(4)\n\t" \
    "v_mfma_f32_32x32x16_bf16 v[0:15], v[188:191], v[164:167], v[0:15]\n\t" \
    "ds_read_b64_tr_b16 v[176:177], v234 offset:0\n\t" \
    "ds_read_b64_tr_b16 v[178:179], v234 offset:768\n\t" \
    "s_waitcnt lgkmcnt(4)\n\t" \
    "v_mfma_f32_32x32x16_bf16 v[16:31], v[192:195], v[164:167], v[16:31]\n\t" \
    "ds_read_b64_tr_b16 v[180:181], v234 offset:64\n\t" \
    "ds_read_b64_tr_b16 v[182:183], v234 offset:832\n\t" \
    "s_waitcnt lgkmcnt(4)\n\t" \
    "v_mfma_f32_32x32x16_bf16 v[32:47], v[196:199], v[164:167], v[32:47]\n\t" \
    "ds_read_b64_tr_b16 v[184:185], v234 offset:128\n\t" \
    "ds_read_b64_tr_b16 v[186:187], v234 offset:896\n\t" \
    "v_cvt_pk_bf16_f32 v209, v208, 0\n\t" \
    "v_cvt_pk_bf16_f32 v213, v212, 0\n\t" \
    "v_cvt_pk_bf16_f32 v217, v216, 0\n\t" \
    "v_lshlrev_b32 v210, 16, v209\n\t" \
    "v_lshlrev_b32 v214, 16, v213\n\t" \
    "v_lshlrev_b32 v218, 16, v217\n\t" \
    "v_sub_f32 v208, v208, v210\n\t" \
    "v_sub_f32 v212, v212, v214\n\t" \
    "v_sub_f32 v216, v216, v218\n\t" \
    "v_cvt_pk_bf16_f32 v208, v210, v208\n\t" \
    "v_cvt_pk_bf16_f32 v212, v214, v212\n\t" \
    "v_cvt_pk_bf16_f32 v216, v218, v216\n\t" \
    "s_mov_b32 exec_hi, 0\n\t" \
    "ds_write_b32 v206, v208 offset:2048\n\t" \
    "ds_write_b32 v206, v212 offset:2560\n\t" \
    "ds_write_b32 v206, v216 offset:3072\n\t" \
    "s_mov_b32 exec_hi, -1\n\t" \
    "s_mov_b64 exec, 1\n\t" \
    "ds_add_u32 v205, v220 offset:0\n\t" \
    "s_mov_b64 exec, -1\n\t" \
    "s_waitcnt lgkmcnt(4)\n\t" \
    "v_mfma_f32_32x32x16_bf16 v[0:15], v[176:179], v[168:171], v[0:15]\n\t" \
    "ds_read_b64_tr_b16 v[188:189], v235 offset:0\n\t" \
    "ds_read_b64_tr_b16 v[190:191], v235 offset:768\n\t" \
    "s_waitcnt lgkmcnt(4)\n\t" \
    "v_mfma_f32_32x32x16_bf16 v[16:31], v[180:183], v[168:171], v[16:31]\n\t" \
    "ds_read_b64_tr_b16 v[192:193], v235 offset:64\n\t" \
    "ds_read_b64_tr_b16 v[194:195], v235 offset:832\n\t" \
    "s_waitcnt lgkmcnt(4)\n\t" \
    "v_mfma_f32_32x32x16_bf16 v[32:47], v[184:187], v[168:171], v[32:47]\n\t" \
    "ds_read_b64_tr_b16 v[196:197], v235 offset:128\n\t" \
    "ds_read_b64_tr_b16 v[198:199], v235 offset:896\n\t" \
    "s_waitcnt lgkmcnt(4)\n\t" \
    "v_mfma_f32_32x32x16_bf16 v[0:15], v[188:191], v[172:175], v[0:15]\n\t" \
    "14:\n\t" \
    "ds_read_b32 v204, v205 offset:0\n\t" \
    "s_waitcnt lgkmcnt(0)\n\t" \
    "v_readfirstlane_b32 %[tmp], v204\n\t" \
    "s_nop 1\n\t" \
    "s_cmp_lt_u32 %[tmp], 16\n\t" \
    "s_cbranch_scc1 14b\n\t" \
    "ds_read_b128 v[176:179], v207 offset:2048\n\t" \
    "ds_read_b128 v[180:183], v207 offset:2560\n\t" \
    "ds_read_b128 v[184:187], v207 offset:3072\n\t" \
    "v_mfma_f32_32x32x16_bf16 v[16:31], v[192:195], v[172:175], v[16:31]\n\t" \
    "v_mfma_f32_32x32x16_bf16 v[32:47], v[196:199], v[172:175], v[32:47]\n\t" \
    "s_waitcnt lgkmcnt(2)\n\t" \
    "v_mfma_f32_32x32x16_bf16 v[0:15], v[176:179], v[236:239], v[0:15]\n\t" \
    "s_waitcnt lgkmcnt(1)\n\t" \
    "v_mfma_f32_32x32x16_bf16 v[16:31], v[180:183], v[236:239], v[16:31]\n\t" \
    "s_waitcnt lgkmcnt(0)\n\t" \
    "v_mfma_f32_32x32x16_bf16 v[32:47], v[184:187], v[236:239], v[32:47]\n\t" \
    "s_nop 11\n\t"
#define K1B2_CLOB "v176", "v177", "v178", "v179", "v180", "v181", "v182", "v183", "v184", "v185", "v186", "v187", "v188", "v189", "v190", "v191", "v192", "v193", "v194", "v195", "v196", "v197", "v198", "v199", "v204", "v220", "v208", "v209", "v210", "v211", "v212", "v213", "v214", "v215", "v216", "v217", "v218", "v219"
constexpr int K1_Z1W = 98304;
constexpr int K1_CSP = K1_Z1W + 57344;
constexpr int K1_STAT = K1_CSP + 3584;
constexpr int K1_CNT = K1_STAT + 1536;
constexpr int K1_LDS = K1_CNT + 16;
typedef __attribute__((ext_vector_type(32))) float f32x32;
typedef __attribute__((ext_vector_type(16))) unsigned u32x16;
typedef __attribute__((ext_vector_type(8))) unsigned u32x8;

#define PINV(x) asm volatile("" : "+v"(x))
template <int D> DI void st16x2(char* base, unsigned off, u32x4 v0, u32x4 v1) {
    asm volatile("global_store_dwordx4 %0, %1, %3 sc1\n\tglobal_store_dwordx4 %0, %2, %3 offset:%4 sc1\n\ts_nop 1"
                 :: "v"(off), "v"(v0), "v"(v1), "s"(base), "i"(D) : "memory");
}
DI void st16(char* base, unsigned off, u32x4 v) {
    asm volatile("global_store_dwordx4 %0, %1, %2 sc1\n\ts_nop 1" :: "v"(off), "v"(v), "s"(base) : "memory");
}
DI int lane_id() { int l; asm volatile("v_mbcnt_lo_u32_b32 %0, -1, 0\n\tv_mbcnt_hi_u32_b32 %0, -1, %0" : "=v"(l)); return l; }
DI float fmul1(float a, float b) { float r; asm("v_mul_f32 %0, %1, %2" : "=v"(r) : "v"(a), "v"(b)); return r; }
DI float fadd1(float a, float b) { float r; asm("v_add_f32 %0, %1, %2" : "=v"(r) : "v"(a), "v"(b)); return r; }
DI float ffma1(float a, float b, float c) { float r; asm("v_fma_f32 %0, %1, %2, %3" : "=v"(r) : "v"(a), "v"(b), "v"(c)); return r; }

typedef __attribute__((address_space(1))) unsigned gu32;
typedef __attribute__((address_space(1))) u64 gu64;
#define RLX_AGENT __ATOMIC_RELAXED, __HIP_MEMORY_SCOPE_AGENT

__global__ __launch_bounds__(512, 2) void k1_proj(const u16* __restrict__ x, const u16* __restrict__ wqkv, const u16* __restrict__ win,
                                                  const u16* __restrict__ wo, const u16* __restrict__ wout, const u16* __restrict__ wsp, const u16* __restrict__ bsp,
                                                  char* __restrict__ ws) {
    extern __shared__ __attribute__((aligned(16))) char smem[];
    const int w = __builtin_amdgcn_readfirstlane(threadIdx.x >> 6);
    const unsigned xcd = blockIdx.x & 7, idx = blockIdx.x >> 3;

    unsigned cpf;
    asm volatile("s_cmp_lt_u32 %1, 2\n\ts_cbranch_scc0 1f\n\t"
                 "s_getpc_b64 vcc\n\t.Lcode_here:\n\t"
                 "v_lshlrev_b32 v242, 7, %2\n\tv_min_u32 v241, .Lcode_tail-.Lcode_here-32768, v242\n\tv_add_u32 v241, 0x8000, v241\n\t"
                 "global_load_dword %0, v242, vcc\n\tglobal_load_dword %0, v241, vcc\n\t1:"
                 : "={v243}"(cpf) : "s"(idx + (w & 4) * 64), "v"(threadIdx.x) : "vcc", "scc", "v241", "v242");
    const unsigned i7 = (idx * 37u) >> 8, m7 = idx - 7u * i7;
    const int rt = 4 * (xcd >> 1) + i7, j = 7 * (xcd & 1) + m7;
    const int rg = w & 3;
    const unsigned lds0 = (unsigned)(uintptr_t)smem;
    const unsigned ONES = 0x3f803f80u;
    const int stile = rt * 4 + rg;
    const char* xb = (const char*)(x + (size_t)(rt * 128 + rg * 32) * 448);
    const char* wab = (const char*)(win + (size_t)(rg * 16) * 1792 + 896 + 64 * j);
#define LANE_DEFS const int lane = lane_id(), tid = w * 64 + lane, r = lane & 31, h = lane >> 5, blk = (lane >> 4) & 1, q4 = (lane & 15) >> 2, p4 = lane & 3; \
    const int xrow = rg * 32 + r; const unsigned wsw = (r >> 1) & 7; (void)tid; (void)blk; (void)q4; (void)p4; (void)xrow; (void)wsw
#define DMA_DEFS u32x4 dvx; u32x2 dvw; \
    _Pragma("unroll") for (int k = 0; k < 4; ++k) dvx[k] = (8 * k + (lane >> 3)) * 896 + 16 * ((lane & 7) ^ ((4 * k + (lane >> 4)) & 7)); \
    dvw[0] = (lane >> 3) * 3584 + 16 * ((lane & 7) ^ (4 * blk)); dvw[1] = dvw[0] + 8 * 3584
    if (__builtin_expect(w >= 4 && idx < 28, 1)) {
        __builtin_amdgcn_s_setprio(2);
        __builtin_amdgcn_s_waitcnt(0x0F70);
        LANE_DEFS; DMA_DEFS;
        f32x32 xf0, xf1, xf2; f32x16 xf3; float sx, sxx;

        asm volatile(K1B0_ASM
            : "+{s[40:41]}"(xb), "+{s[42:43]}"(wab)
            : "{v[216:219]}"(dvx), "{v[220:221]}"(dvw), [lx] "s"(lds0 + rg * 4096), [lw] "s"(lds0 + 16384 + rg * 2048)
            : "memory", "scc");
        if (tid < 260) ((unsigned*)(smem + K1_CNT))[tid - 256] = 0u;
        const char* wbb = (const char*)(wqkv + (size_t)(rg * 32) * 1344 + 96 * j);
        u32x4 wbv; u32x8 xa, wb;
#pragma unroll
        for (int m = 0; m < 3; ++m) { const int i = 64 * m + lane; wbv[m] = (i / 12) * 2688 + (i % 12) * 16; }
        wbv[3] = 0;
#pragma unroll
        for (int i = 0; i < 4; ++i) { xa[i] = lds0 + xrow * 128 + 16 * ((2 * ((i + rg) & 3) + h) ^ wsw); xa[4 + i] = xa[i] + 49152; }
#pragma unroll
        for (int i = 0; i < 4; ++i) { wb[i] = lds0 + ((i + rg) & 3) * 3072 + (8 * h + q4) * 192 + 32 * blk + 8 * p4; wb[4 + i] = wb[i] + 49152; }
        const unsigned selv = r == (blk ? 20 : 0) ? ONES : 0u;
        const u32x4 sel = {selv, selv, selv, selv};
        unsigned dst[3][2];
#pragma unroll
        for (int t = 0; t < 3; ++t)
#pragma unroll
            for (int s = 0; s < 2; ++s) {
                const unsigned o8 = 12 * j + 4 * t + 2 * s + h, hp = (o8 * 9363u) >> 16, m = o8 - 7 * hp, hd = (2 * hp + (m > 3 ? 1 : 0)) & 15;
                dst[t][s] = (hp < 8 ? (unsigned)WS_QF + stile * 2048 + (hd << 17) : (unsigned)WS_KV + stile * 4096 + (hd << 18)) + (m & 3) * 512 + r * 16 + (m == 3 ? 1u : 0u);
                PINV(dst[t][s]);
            }
        asm volatile(K1B1_ASM
            : "={v[64:95]}"(xf0), "={v[96:127]}"(xf1), "={v[128:159]}"(xf2), "={v[160:175]}"(xf3), "={v200}"(sx), "={v204}"(sxx),
              "+{s[40:41]}"(xb), "+{s[42:43]}"(wab), "+{s[44:45]}"(wbb), "+{v[228:235]}"(wb)
            : "{v[192:195]}"(sel), "{v[208:215]}"(xa), "{v[216:219]}"(dvx), "{v[220:221]}"(dvw), "{v[224:227]}"(wbv),
              [lx] "s"(lds0 + rg * 4096), [lw] "s"(lds0 + 16384 + rg * 2048), [lb] "s"(lds0 + rg * 6144)
            : "memory", "scc", K1B1_CLOB);
        sx = sum32(sx); sxx = sum32(sxx);
        const float mu = fmul1(sx, 1.f / 448.f);
        const float rs = __builtin_amdgcn_rsqf(fmaxf(sxx * (1.f / 448.f) - mu * mu, 0.f) + 1e-5f);
        u32x4 mfrag;
        { const unsigned mh = pk2(-mu, -mu); const float ml = -mu - bf_lo(mh); const unsigned mm = h ? pk2(ml, ml) : mh; mfrag = u32x4{mm, mm, mm, mm}; }
        *(unsigned*)(smem + K1_STAT + rg * 256 + lane * 4) = mfrag[0];
        *(float*)(smem + K1_STAT + 1024 + xrow * 4) = rs;
        f32x16 acc[3];
        {
            int tmp;

            asm volatile(K1B2_ASM
                : "={v[0:15]}"(acc[0]), "={v[16:31]}"(acc[1]), "={v[32:47]}"(acc[2]), "+{s[44:45]}"(wbb), [tmp] "=&s"(tmp)
                : "{v[64:95]}"(xf0), "{v[96:127]}"(xf1), "{v[128:159]}"(xf2), "{v[160:175]}"(xf3),
                  "{v205}"(lds0 + K1_CNT), "{v206}"(lds0 + K1_CSP + r * 16 + rg * 4), "{v[200:203]}"(sel),
                  "{v207}"(lds0 + K1_CSP + r * 16), "{v[236:239]}"(mfrag),
                  "{v[224:227]}"(wbv), "{v[228:235]}"(wb),
                  [lb3] "s"(lds0 + rg * 3072), [rgo] "s"(rg * 16 * 2688)
                : "memory", "scc", K1B2_CLOB);

        }
        if (j == 0) ((float*)(ws + WS_RS))[(rt * 128 + xrow) * 2] = rs;
        char* scr = smem + 3 * 24576 + rg * 2048;
#pragma unroll
        for (int t = 0; t < 3; ++t) {
            const int ct = 3 * j + t;
            const float sc = ct < 14 ? rs * (0.18898224f * 1.44269504f) : rs;
            unsigned pkd[8];
#pragma unroll
            for (int g = 0; g < 8; ++g) pkd[g] = pk2(fmul1(acc[t][2 * g], sc), fmul1(acc[t][2 * g + 1], sc));
            if (ct < 28) {
#pragma unroll
                for (int s = 0; s < 2; ++s) {
                    auto s0 = __builtin_amdgcn_permlane32_swap(pkd[4 * s], pkd[4 * s + 2], false, false);
                    auto s1 = __builtin_amdgcn_permlane32_swap(pkd[4 * s + 1], pkd[4 * s + 3], false, false);
                    const bool sp = dst[t][s] & 1u;
                    const unsigned off = dst[t][s] & ~1u;
                    st16(ws, off, u32x4{s0[0], s1[0], sp ? ONES : s0[1], sp ? 0x00003f80u : s1[1]});
                    if (sp) st16(ws, off + (ct < 14 ? 1u << 17 : 1u << 18), u32x4{s0[1], s1[1], ONES, 0x00003f80u});
                }
            } else {
#pragma unroll
                for (int g = 0; g < 4; ++g) *(u32x2*)(scr + r * 64 + 8 * ((2 * g + h) ^ wsw)) = u32x2{pkd[2 * g], pkd[2 * g + 1]};
                const unsigned n = 32 * (ct - 28) + r, hd = (n * 2341u) >> 16, d = n - 28 * hd;
                const unsigned dpo = (unsigned)WS_KV + stile * 4096 + (hd << 18) + 2048 + (d + 32 * h) * 16;
                u32x4 vf[2];
#pragma unroll
                for (int s = 0; s < 2; ++s) {
                    const int r0 = 16 * s + 4 * h + q4, r1 = r0 + 8;
                    const s16x4 lo = trread((const LAS char*)scr + r0 * 64 + 8 * ((4 * blk + p4) ^ ((r0 >> 1) & 7)));
                    const s16x4 hi = trread((const LAS char*)scr + r1 * 64 + 8 * ((4 * blk + p4) ^ ((r1 >> 1) & 7)));
                    vf[s] = __builtin_bit_cast(u32x4, cat8(lo, hi));
                }
                st16x2<1024>(ws, dpo, vf[0], vf[1]);
            }
        }
    } else if (__builtin_expect(idx < 28, 1)) {
        gu32* tick = (gu32*)(ws + WS_TK) + stile * 32;
        unsigned tk = 1u;
        xb += 256; wab += 128 * 3584;
        f32x16 acc[4]; f32x32 xf0; unsigned tag, rsb, wt;
        {
            LANE_DEFS; DMA_DEFS;
            rsb = lds0 + K1_STAT + 1024 + xrow * 4;
            wt = ((rt * 14 + j) * 256 + tid) * 128;
            unsigned mw = lds0 + K1_STAT + rg * 256 + lane * 4;
            const unsigned selv = r == (blk ? 20 : 0) ? ONES : 0u;
            const u32x4 sel = {selv, selv, selv, selv};
            const char* wzb = (const char*)(win + (size_t)(rg * 16) * 1792 + 64 * j);
            const unsigned li = (rt * 14 + j) * 162 + min(tid, 161);
            const char* tp = li < 14336 ? (const char*)x + li * 128u : li < 26880 ? (const char*)win + (li - 14336) * 128u : (const char*)wqkv + (li - 26880) * 128u;
            u32x8 xa, wa0, wa1; int tmp;
#pragma unroll
            for (int i = 0; i < 4; ++i) { xa[i] = lds0 + xrow * 128 + 16 * ((2 * ((i + rg) & 3) + h) ^ wsw); xa[4 + i] = xa[i] + 49152; }
#pragma unroll
            for (int i = 0; i < 4; ++i)
#pragma unroll
                for (int t = 0; t < 2; ++t) {
                    wa0[2 * i + t] = lds0 + (16 * ((i + rg) & 3) + 8 * h + q4) * 128 + 64 * (t ^ (q4 >> 1)) + 32 * blk + 8 * p4;
                    wa1[2 * i + t] = wa0[2 * i + t] + 49152;
                }

            asm volatile(K1A_ASM
                : "={v[0:15]}"(acc[0]), "={v[16:31]}"(acc[1]), "={v[32:47]}"(acc[2]), "={v[48:63]}"(acc[3]), "={v[64:95]}"(xf0),
                  "+{v235}"(tk), "+{v237}"(wt), "+{v238}"(mw), "+{v240}"(rsb), "+{v[204:211]}"(wa0), "+{v[212:219]}"(wa1), "+{v[220:227]}"(xa), "+{v[228:231]}"(dvx),
                  "+{s[46:47]}"(wzb), "+{v[252:253]}"(tp), "+{v243}"(cpf), [tag] "=&s"(tag), [tmp] "=&s"(tmp)
                : "{v[200:203]}"(sel), "{v[232:233]}"(dvw), "{v234}"(lds0 + K1_CSP + r * 16 + rg * 4), "{v236}"(0u),
                  "{v239}"(lds0 + K1_CSP + r * 16), "{v241}"(lane * 8), "{v242}"(lds0 + K1_CNT), "{s[40:41]}"(xb), "{s[42:43]}"(wab),
                  [tick] "s"(tick), [wsp] "s"(wsp), [lx] "s"(lds0 + rg * 4096), [lw] "s"(lds0 + 16384 + rg * 2048),
                  [slots] "s"(ws + WS_ST + (size_t)stile * 7168 + 3584), [pub] "s"(ws + WS_ST + (size_t)stile * 7168 + j * 512)
                : "memory", "scc", K1A_CLOB);

        }
        LANE_DEFS;
        const float rs = __uint_as_float(rsb);
        gu64* slots = (gu64*)(ws + WS_ST) + (size_t)stile * (14 * 64) + lane;
        {
            u32x4 zp[4];
#pragma unroll
            for (int k = 0; k < 4; ++k) { const int t = 2 + (k >> 1), s = k & 1;
                zp[k] = u32x4{pk2(acc[t][4 * s], acc[t][4 * s + 1]), pk2(acc[t][4 * s + 2], acc[t][4 * s + 3]),
                              pk2(acc[t][8 + 4 * s], acc[t][8 + 4 * s + 1]), pk2(acc[t][8 + 4 * s + 2], acc[t][8 + 4 * s + 3])}; }
            char* zb = ws + WS_Z1 + (size_t)(stile * 56 + 4 * j) * 1024;
            st16x2<1024>(zb, lane * 16, zp[0], zp[1]);
            st16x2<1024>(zb + 2048, lane * 16, zp[2], zp[3]);
        }
        float tot;
        {
            asm volatile("s_waitcnt vmcnt(4)" : "+{v[64:95]}"(xf0), "+{v237}"(wt) :: "memory");
            unsigned gv[14], gt[14];
#pragma unroll
            for (int p = 0; p < 14; ++p) { gv[p] = __float_as_uint(xf0[2 * p]); gt[p] = __float_as_uint(xf0[2 * p + 1]); }
            for (unsigned spins = 0;; ++spins) {
                unsigned mx = 0u; tot = 0.f;
#pragma unroll
                for (int p = 0; p < 14; ++p) { mx = max(mx, gt[p] - (tag - 13u)); tot += __uint_as_float(gv[p]); }
                if (__all(mx <= 26u) || spins > (1u << 22)) break;
                if (spins) __builtin_amdgcn_s_sleep(4);
#pragma unroll
                for (int p = 0; p < 14; ++p) { const u64 g = __hip_atomic_load(slots + p * 64, RLX_AGENT); gv[p] = (unsigned)g; gt[p] = (unsigned)(g >> 32); }
            }
        }

        if (lane == 0) __hip_atomic_fetch_add(tick, 1024u, RLX_AGENT);
        {
            auto sw = __builtin_amdgcn_permlane32_swap(__float_as_uint(tot), __float_as_uint(tot), false, false);
            const float m2 = __uint_as_float(sw[0]) * (1.f / 896.f);
            const float v2 = fmaxf(__uint_as_float(sw[1]) * (1.f / 896.f) - m2 * m2, 0.f);
            const float sc = rs * __builtin_amdgcn_rsqf(fmaf(v2 * rs, rs, 1e-5f)), of = -m2 * sc;
            u32x4 zf[2][2];
#pragma unroll
            for (int t = 0; t < 2; ++t) {
                char* scr = smem + K1_Z1W + rg * 4096 + t * 2048;
#pragma unroll
                for (int g = 0; g < 4; ++g)
                    *(u32x2*)(scr + r * 64 + 8 * ((2 * g + h) ^ wsw)) =
                        u32x2{pk2(ffma1(acc[t][4 * g], sc, of), ffma1(acc[t][4 * g + 1], sc, of)), pk2(ffma1(acc[t][4 * g + 2], sc, of), ffma1(acc[t][4 * g + 3], sc, of))};
#pragma unroll
                for (int s = 0; s < 2; ++s) {
                    const int r0 = 16 * s + 8 * h + q4, r1 = r0 + 4;
                    const s16x4 lo = trread((const LAS char*)scr + r0 * 64 + 8 * ((4 * blk + p4) ^ ((r0 >> 1) & 7)));
                    const s16x4 hi = trread((const LAS char*)scr + r1 * 64 + 8 * ((4 * blk + p4) ^ ((r1 >> 1) & 7)));
                    zf[t][s] = __builtin_bit_cast(u32x4, cat8(lo, hi));
                }
            }
#pragma unroll
            for (int t = 0; t < 2; ++t) {
                const unsigned un = 4 * j + 2 * t + blk, ft = (un * 37u) >> 8, ul = un - 7 * ft;
                const unsigned lu = (ft & 1) ? (ul == 0 ? 6u : ul - 1) : ul;
                st16x2<512>(ws, (unsigned)WS_Z2 + (ft * 64 + stile) * 7168 + lu * 1024 + ((lane & 15) + 16 * h) * 16, zf[t][0], zf[t][1]);
            }
        }
        __builtin_amdgcn_endpgm();
    } else {
        LANE_DEFS;
        const int sp = xcd * 4 + (idx - 28);
        if (tid < 64) { *(unsigned*)(ws + WS_FL + (sp * 64 + tid) * FLS) = 0u; ((float*)(ws + WS_RS))[(sp * 64 + tid) * 2 + 1] = bf_lo(bsp[sp * 64 + tid]); }
        { const unsigned e = sp * 512 + tid, c = (e & 3) ? 0u : ONES;
          *(u32x4*)(ws + WS_KV + (e >> 4) * 4096 + 2048 + ((e >> 2) & 3) * 512 + (28 + (e & 3)) * 16) = u32x4{c, c, c, c}; }
        const unsigned wt = *(const unsigned*)((const char*)wsp + (57344 + sp * 256 + (tid & 255)) * 128u);
        u16 e[5][8];
#pragma unroll
        for (int it = 0; it < 5; ++it) {
            const int fr = min(sp * 8 + w + 256 * it, 14 * 88 - 1);
            const int ct = fr / 88, ks = fr - 88 * ct;
            const int q = ks - 32, ft = q / 7, s = q - 7 * ft;
            int row, hi;
            if (ks < 32) { row = 28 * (ks >> 1) + 16 * (ks & 1) + 4 * h; hi = ((ks & 1) && h) ? 0 : 8; }
            else if (s < 6) { row = 16 * (7 * ft + (ft & 1) + (s & 6)) + 8 * (s & 1) + 4 * h; hi = 16; }
            else { row = 16 * (7 * ft + ((ft & 1) ? 0 : 6)) + 8 * h; hi = 4; }
            const u16* src = (ks < 32 ? wo : wout) + row * 448 + 32 * ct + r;
#pragma unroll
            for (int jj = 0; jj < 8; ++jj) e[it][jj] = src[((jj & 3) + (jj >> 2) * hi) * 448];
        }
        SBAR();
#pragma unroll
        for (int it = 0; it < 5; ++it) {
            const int fr = min(sp * 8 + w + 256 * it, 14 * 88 - 1);
            const int ks = fr % 88;
            const unsigned keep = (ks < 32 && (ks & 1) && h) ? 0u : ~0u;
            *(u32x4*)(ws + WS_WC + (size_t)fr * 1024 + lane * 16) =
                u32x4{e[it][0] | ((unsigned)e[it][1] << 16), e[it][2] | ((unsigned)e[it][3] << 16),
                      (e[it][4] | ((unsigned)e[it][5] << 16)) & keep, (e[it][6] | ((unsigned)e[it][7] << 16)) & keep};
        }
        asm volatile("" :: "v"(wt));
        return;
    }

}

constexpr int K2_RING = 0;
constexpr int K2_ACMB = 98304;
constexpr int K2_AML = K2_ACMB + 49152;
constexpr int K2_CNT = K2_AML + 8192;
constexpr int K2_LDS = K2_CNT + 16;
#define ATT_ASM \
    "global_load_dwordx4 v[192:195], %[vo], %[kv] offset:-4096\n\t" \
    "global_load_dwordx4 v[196:199], %[vo], %[kv] offset:-3072\n\t" \
    "s_cmp_lg_u32 %[slow], 0\n\t" \
    "s_cbranch_scc1 2f\n\t" \
    "v_xor_b32 v100, 6144, %[qo]\n\t" \
    "global_load_dwordx4 v[64:67], v100, %[qb]\n\t" \
    "global_load_dwordx4 v[68:71], v100, %[qb] offset:1024\n\t" \
    "v_xor_b32 v101, 4096, %[qo]\n\t" \
    "global_load_dwordx4 v[72:75], v101, %[qb]\n\t" \
    "global_load_dwordx4 v[76:79], v101, %[qb] offset:1024\n\t" \
    "v_xor_b32 v102, 2048, %[qo]\n\t" \
    "global_load_dwordx4 v[80:83], v102, %[qb]\n\t" \
    "global_load_dwordx4 v[84:87], v102, %[qb] offset:1024\n\t" \
    "global_load_dwordx4 v[88:91], %[qo], %[qb]\n\t" \
    "global_load_dwordx4 v[92:95], %[qo], %[qb] offset:1024\n\t" \
    "s_waitcnt lgkmcnt(0)\n\t" \
    "s_barrier\n\t" \
    "2:\n\t" \
    "global_load_dwordx4 v[200:203], %[vo], %[kv] offset:-2048\n\t" \
    "global_load_dwordx4 v[204:207], %[vo], %[kv] offset:-1024\n\t" \
    "global_load_dwordx4 v[208:211], %[vo], %[kv] offset:0\n\t" \
    "global_load_dwordx4 v[212:215], %[vo], %[kv] offset:1024\n\t" \
    "global_load_dwordx4 v[216:219], %[vo], %[kv] offset:2048\n\t" \
    "global_load_dwordx4 v[220:223], %[vo], %[kv] offset:3072\n\t" \
    "v_add_u32 %[vo], 0x2000, %[vo]\n\t" \
    "global_load_dwordx4 v[224:227], %[vo], %[kv] offset:-4096\n\t" \
    "global_load_dwordx4 v[228:231], %[vo], %[kv] offset:-3072\n\t" \
    "global_load_dwordx4 v[232:235], %[vo], %[kv] offset:-2048\n\t" \
    "global_load_dwordx4 v[236:239], %[vo], %[kv] offset:-1024\n\t" \
    "global_load_dwordx4 v[240:243], %[vo], %[kv] offset:0\n\t" \
    "global_load_dwordx4 v[244:247], %[vo], %[kv] offset:1024\n\t" \
    "v_add_u32 %[vo], 0x1800, %[vo]\n\t" \
    "v_mov_b32 v0, 0\n\t" \
    "v_mov_b32 v1, 0\n\t" \
    "v_mov_b32 v2, 0\n\t" \
    "v_mov_b32 v3, 0\n\t" \
    "v_mov_b32 v4, 0\n\t" \
    "v_mov_b32 v5, 0\n\t" \
    "v_mov_b32 v6, 0\n\t" \
    "v_mov_b32 v7, 0\n\t" \
    "v_mov_b32 v8, 0\n\t" \
    "v_mov_b32 v9, 0\n\t" \
    "v_mov_b32 v10, 0\n\t" \
    "v_mov_b32 v11, 0\n\t" \
    "v_mov_b32 v12, 0\n\t" \
    "v_mov_b32 v13, 0\n\t" \
    "v_mov_b32 v14, 0\n\t" \
    "v_mov_b32 v15, 0\n\t" \
    "v_mov_b32 v16, 0\n\t" \
    "v_mov_b32 v17, 0\n\t" \
    "v_mov_b32 v18, 0\n\t" \
    "v_mov_b32 v19, 0\n\t" \
    "v_mov_b32 v20, 0\n\t" \
    "v_mov_b32 v21, 0\n\t" \
    "v_mov_b32 v22, 0\n\t" \
    "v_mov_b32 v23, 0\n\t" \
    "v_mov_b32 v24, 0\n\t" \
    "v_mov_b32 v25, 0\n\t" \
    "v_mov_b32 v26, 0\n\t" \
    "v_mov_b32 v27, 0\n\t" \
    "v_mov_b32 v28, 0\n\t" \
    "v_mov_b32 v29, 0\n\t" \
    "v_mov_b32 v30, 0\n\t" \
    "v_mov_b32 v31, 0\n\t" \
    "v_mov_b32 v32, 0\n\t" \
    "v_mov_b32 v33, 0\n\t" \
    "v_mov_b32 v34, 0\n\t" \
    "v_mov_b32 v35, 0\n\t" \
    "v_mov_b32 v36, 0\n\t" \
    "v_mov_b32 v37, 0\n\t" \
    "v_mov_b32 v38, 0\n\t" \
    "v_mov_b32 v39, 0\n\t" \
    "v_mov_b32 v40, 0\n\t" \
    "v_mov_b32 v41, 0\n\t" \
    "v_mov_b32 v42, 0\n\t" \
    "v_mov_b32 v43, 0\n\t" \
    "v_mov_b32 v44, 0\n\t" \
    "v_mov_b32 v45, 0\n\t" \
    "v_mov_b32 v46, 0\n\t" \
    "v_mov_b32 v47, 0\n\t" \
    "v_mov_b32 v48, 0\n\t" \
    "v_mov_b32 v49, 0\n\t" \
    "v_mov_b32 v50, 0\n\t" \
    "v_mov_b32 v51, 0\n\t" \
    "v_mov_b32 v52, 0\n\t" \
    "v_mov_b32 v53, 0\n\t" \
    "v_mov_b32 v54, 0\n\t" \
    "v_mov_b32 v55, 0\n\t" \
    "v_mov_b32 v56, 0\n\t" \
    "v_mov_b32 v57, 0\n\t" \
    "v_mov_b32 v58, 0\n\t" \
    "v_mov_b32 v59, 0\n\t" \
    "v_mov_b32 v60, 0\n\t" \
    "v_mov_b32 v61, 0\n\t" \
    "v_mov_b32 v62, 0\n\t" \
    "v_mov_b32 v63, 0\n\t" \
    "v_mov_b32 v180, 0\n\t" \
    "v_mov_b32 v184, 0\n\t" \
    "v_mov_b32 v252, 0\n\t" \
    "v_mov_b32 v181, 0\n\t" \
    "v_mov_b32 v185, 0\n\t" \
    "v_mov_b32 v253, 0\n\t" \
    "v_mov_b32 v182, 0\n\t" \
    "v_mov_b32 v186, 0\n\t" \
    "v_mov_b32 v254, 0\n\t" \
    "v_mov_b32 v183, 0\n\t" \
    "v_mov_b32 v187, 0\n\t" \
    "v_mov_b32 v255, 0\n\t" \
    "s_waitcnt vmcnt(12)\n\t" \
    "s_cbranch_scc1 3f\n\t" \
    "s_mov_b32 exec_lo, 0\n\t" \
    "v_mov_b32 v70, 0\n\t" \
    "v_mov_b32 v71, 0\n\t" \
    "v_mov_b32 v78, 0\n\t" \
    "v_mov_b32 v79, 0\n\t" \
    "v_mov_b32 v86, 0\n\t" \
    "v_mov_b32 v87, 0\n\t" \
    "v_mov_b32 v94, 0\n\t" \
    "v_mov_b32 v95, 0\n\t" \
    "s_mov_b32 exec_lo, -1\n\t" \
    "v_mfma_f32_32x32x16_bf16 v[144:159], v[192:195], v[64:67], 0\n\t" \
    "v_mfma_f32_32x32x16_bf16 v[144:159], v[196:199], v[68:71], v[144:159]\n\t" \
    "v_mfma_f32_32x32x16_bf16 v[160:175], v[192:195], v[72:75], 0\n\t" \
    "v_mfma_f32_32x32x16_bf16 v[160:175], v[196:199], v[76:79], v[160:175]\n\t" \
    "v_mfma_f32_32x32x16_bf16 v[100:115], v[192:195], v[80:83], 0\n\t" \
    "v_mfma_f32_32x32x16_bf16 v[100:115], v[196:199], v[84:87], v[100:115]\n\t" \
    "v_mfma_f32_32x32x16_bf16 v[116:131], v[192:195], v[88:91], 0\n\t" \
    "v_mfma_f32_32x32x16_bf16 v[116:131], v[196:199], v[92:95], v[116:131]\n\t" \
    "s_nop 5\n\t" \
    "v_max3_f32 v144, v144, v145, v146\n\t" \
    "v_max3_f32 v147, v147, v148, v149\n\t" \
    "v_max3_f32 v150, v150, v151, v152\n\t" \
    "v_max3_f32 v153, v153, v154, v155\n\t" \
    "v_max3_f32 v156, v156, v157, v158\n\t" \
    "v_max3_f32 v144, v144, v147, v150\n\t" \
    "v_max3_f32 v153, v153, v156, v159\n\t" \
    "v_max3_f32 v160, v160, v161, v162\n\t" \
    "v_max3_f32 v163, v163, v164, v165\n\t" \
    "v_max3_f32 v166, v166, v167, v168\n\t" \
    "v_max3_f32 v169, v169, v170, v171\n\t" \
    "v_max3_f32 v172, v172, v173, v174\n\t" \
    "v_max3_f32 v160, v160, v163, v166\n\t" \
    "v_max3_f32 v169, v169, v172, v175\n\t" \
    "v_max3_f32 v100, v100, v101, v102\n\t" \
    "v_max3_f32 v103, v103, v104, v105\n\t" \
    "v_max3_f32 v106, v106, v107, v108\n\t" \
    "v_max3_f32 v109, v109, v110, v111\n\t" \
    "v_max3_f32 v112, v112, v113, v114\n\t" \
    "v_max3_f32 v100, v100, v103, v106\n\t" \
    "v_max3_f32 v109, v109, v112, v115\n\t" \
    "v_max3_f32 v116, v116, v117, v118\n\t" \
    "v_max3_f32 v119, v119, v120, v121\n\t" \
    "v_max3_f32 v122, v122, v123, v124\n\t" \
    "v_max3_f32 v125, v125, v126, v127\n\t" \
    "v_max3_f32 v128, v128, v129, v130\n\t" \
    "v_max3_f32 v116, v116, v119, v122\n\t" \
    "v_max3_f32 v125, v125, v128, v131\n\t" \
    "v_max_f32 v144, v144, v153\n\t" \
    "v_max_f32 v160, v160, v169\n\t" \
    "v_max_f32 v100, v100, v109\n\t" \
    "v_max_f32 v116, v116, v125\n\t" \
    "v_sub_f32 v144, 0xc2800000, v144\n\t" \
    "v_sub_f32 v160, 0xc2800000, v160\n\t" \
    "v_sub_f32 v100, 0xc2800000, v100\n\t" \
    "v_sub_f32 v116, 0xc2800000, v116\n\t" \
    "s_mov_b32 exec_lo, 0\n\t" \
    "v_cvt_pk_bf16_f32 v70, v144, 0\n\t" \
    "v_cvt_pk_bf16_f32 v78, v160, 0\n\t" \
    "v_cvt_pk_bf16_f32 v86, v100, 0\n\t" \
    "v_cvt_pk_bf16_f32 v94, v116, 0\n\t" \
    "s_mov_b32 exec_lo, -1\n\t" \
    "v_lshlrev_b32 v96, 16, v70\n\t" \
    "v_lshlrev_b32 v97, 16, v78\n\t" \
    "v_lshlrev_b32 v98, 16, v86\n\t" \
    "v_lshlrev_b32 v99, 16, v94\n\t" \
    "3:\n\t" \
    "s_setprio 0\n\t" \
    "v_mfma_f32_32x32x16_bf16 v[144:159], v[192:195], v[64:67], 0\n\t" \
    "v_mfma_f32_32x32x16_bf16 v[144:159], v[196:199], v[68:71], v[144:159]\n\t" \
    "s_nop 8\n\t" \
    "s_mov_b32 %[cnt], 3\n\t" \
    "1:\n\t" \
    "v_mfma_f32_32x32x16_bf16 v[160:175], v[192:195], v[72:75], 0\n\t" \
    "v_cvt_pk_bf16_f32 v182, v184, v185\n\t" \
    "v_exp_f32 v184, v144\n\t" \
    "v_exp_f32 v185, v145\n\t" \
    "v_cvt_pk_bf16_f32 v183, v186, v187\n\t" \
    "v_exp_f32 v186, v146\n\t" \
    "v_exp_f32 v187, v147\n\t" \
    "v_mfma_f32_32x32x16_bf16 v[48:63], v[252:255], v[180:183], v[48:63]\n\t" \
    "v_cvt_pk_bf16_f32 v176, v184, v185\n\t" \
    "v_exp_f32 v184, v148\n\t" \
    "v_exp_f32 v185, v149\n\t" \
    "v_cvt_pk_bf16_f32 v177, v186, v187\n\t" \
    "v_exp_f32 v186, v150\n\t" \
    "v_exp_f32 v187, v151\n\t" \
    "v_mfma_f32_32x32x16_bf16 v[160:175], v[196:199], v[76:79], v[160:175]\n\t" \
    "v_cvt_pk_bf16_f32 v178, v184, v185\n\t" \
    "v_exp_f32 v184, v152\n\t" \
    "v_exp_f32 v185, v153\n\t" \
    "v_cvt_pk_bf16_f32 v179, v186, v187\n\t" \
    "v_exp_f32 v186, v154\n\t" \
    "v_exp_f32 v187, v155\n\t" \
    "s_waitcnt vmcnt(10)\n\t" \
    "v_mfma_f32_32x32x16_bf16 v[0:15], v[200:203], v[176:179], v[0:15]\n\t" \
    "v_cvt_pk_bf16_f32 v180, v184, v185\n\t" \
    "v_exp_f32 v184, v156\n\t" \
    "v_exp_f32 v185, v157\n\t" \
    "v_cvt_pk_bf16_f32 v181, v186, v187\n\t" \
    "v_exp_f32 v186, v158\n\t" \
    "v_exp_f32 v187, v159\n\t" \
    "s_setprio 3\n\t" \
    "global_load_dwordx4 v[248:251], %[vo], %[kv] offset:-4096\n\t" \
    "global_load_dwordx4 v[252:255], %[vo], %[kv] offset:-3072\n\t" \
    "s_setprio 0\n\t" \
    "v_mfma_f32_32x32x16_bf16 v[144:159], v[192:195], v[80:83], 0\n\t" \
    "v_cvt_pk_bf16_f32 v182, v184, v185\n\t" \
    "v_exp_f32 v184, v160\n\t" \
    "v_exp_f32 v185, v161\n\t" \
    "v_cvt_pk_bf16_f32 v183, v186, v187\n\t" \
    "v_exp_f32 v186, v162\n\t" \
    "v_exp_f32 v187, v163\n\t" \
    "v_mfma_f32_32x32x16_bf16 v[0:15], v[204:207], v[180:183], v[0:15]\n\t" \
    "v_cvt_pk_bf16_f32 v176, v184, v185\n\t" \
    "v_exp_f32 v184, v164\n\t" \
    "v_exp_f32 v185, v165\n\t" \
    "v_cvt_pk_bf16_f32 v177, v186, v187\n\t" \
    "v_exp_f32 v186, v166\n\t" \
    "v_exp_f32 v187, v167\n\t" \
    "v_mfma_f32_32x32x16_bf16 v[144:159], v[196:199], v[84:87], v[144:159]\n\t" \
    "v_cvt_pk_bf16_f32 v178, v184, v185\n\t" \
    "v_exp_f32 v184, v168\n\t" \
    "v_exp_f32 v185, v169\n\t" \
    "v_cvt_pk_bf16_f32 v179, v186, v187\n\t" \
    "v_exp_f32 v186, v170\n\t" \
    "v_exp_f32 v187, v171\n\t" \
    "v_mfma_f32_32x32x16_bf16 v[16:31], v[200:203], v[176:179], v[16:31]\n\t" \
    "v_cvt_pk_bf16_f32 v180, v184, v185\n\t" \
    "v_exp_f32 v184, v172\n\t" \
    "v_exp_f32 v185, v173\n\t" \
    "v_cvt_pk_bf16_f32 v181, v186, v187\n\t" \
    "v_exp_f32 v186, v174\n\t" \
    "v_exp_f32 v187, v175\n\t" \
    "v_mfma_f32_32x32x16_bf16 v[160:175], v[192:195], v[88:91], 0\n\t" \
    "v_cvt_pk_bf16_f32 v182, v184, v185\n\t" \
    "v_exp_f32 v184, v144\n\t" \
    "v_exp_f32 v185, v145\n\t" \
    "v_cvt_pk_bf16_f32 v183, v186, v187\n\t" \
    "v_exp_f32 v186, v146\n\t" \
    "v_exp_f32 v187, v147\n\t" \
    "v_mfma_f32_32x32x16_bf16 v[16:31], v[204:207], v[180:183], v[16:31]\n\t" \
    "v_cvt_pk_bf16_f32 v176, v184, v185\n\t" \
    "v_exp_f32 v184, v148\n\t" \
    "v_exp_f32 v185, v149\n\t" \
    "v_cvt_pk_bf16_f32 v177, v186, v187\n\t" \
    "v_exp_f32 v186, v150\n\t" \
    "v_exp_f32 v187, v151\n\t" \
    "v_mfma_f32_32x32x16_bf16 v[160:175], v[196:199], v[92:95], v[160:175]\n\t" \
    "v_cvt_pk_bf16_f32 v178, v184, v185\n\t" \
    "v_exp_f32 v184, v152\n\t" \
    "v_exp_f32 v185, v153\n\t" \
    "v_cvt_pk_bf16_f32 v179, v186, v187\n\t" \
    "v_exp_f32 v186, v154\n\t" \
    "v_exp_f32 v187, v155\n\t" \
    "v_mfma_f32_32x32x16_bf16 v[32:47], v[200:203], v[176:179], v[32:47]\n\t" \
    "v_cvt_pk_bf16_f32 v180, v184, v185\n\t" \
    "v_exp_f32 v184, v156\n\t" \
    "v_exp_f32 v185, v157\n\t" \
    "v_cvt_pk_bf16_f32 v181, v186, v187\n\t" \
    "v_exp_f32 v186, v158\n\t" \
    "v_exp_f32 v187, v159\n\t" \
    "s_waitcnt vmcnt(10)\n\t" \
    "s_setprio 3\n\t" \
    "global_load_dwordx4 v[192:195], %[vo], %[kv] offset:-2048\n\t" \
    "global_load_dwordx4 v[196:199], %[vo], %[kv] offset:-1024\n\t" \
    "s_setprio 0\n\t" \
    "v_mfma_f32_32x32x16_bf16 v[144:159], v[208:211], v[64:67], 0\n\t" \
    "v_cvt_pk_bf16_f32 v182, v184, v185\n\t" \
    "v_exp_f32 v184, v160\n\t" \
    "v_exp_f32 v185, v161\n\t" \
    "v_cvt_pk_bf16_f32 v183, v186, v187\n\t" \
    "v_exp_f32 v186, v162\n\t" \
    "v_exp_f32 v187, v163\n\t" \
    "v_mfma_f32_32x32x16_bf16 v[32:47], v[204:207], v[180:183], v[32:47]\n\t" \
    "v_cvt_pk_bf16_f32 v176, v184, v185\n\t" \
    "v_exp_f32 v184, v164\n\t" \
    "v_exp_f32 v185, v165\n\t" \
    "v_cvt_pk_bf16_f32 v177, v186, v187\n\t" \
    "v_exp_f32 v186, v166\n\t" \
    "v_exp_f32 v187, v167\n\t" \
    "v_mfma_f32_32x32x16_bf16 v[144:159], v[212:215], v[68:71], v[144:159]\n\t" \
    "v_cvt_pk_bf16_f32 v178, v184, v185\n\t" \
    "v_exp_f32 v184, v168\n\t" \
    "v_exp_f32 v185, v169\n\t" \
    "v_cvt_pk_bf16_f32 v179, v186, v187\n\t" \
    "v_exp_f32 v186, v170\n\t" \
    "v_exp_f32 v187, v171\n\t" \
    "v_mfma_f32_32x32x16_bf16 v[48:63], v[200:203], v[176:179], v[48:63]\n\t" \
    "v_cvt_pk_bf16_f32 v180, v184, v185\n\t" \
    "v_exp_f32 v184, v172\n\t" \
    "v_exp_f32 v185, v173\n\t" \
    "v_cvt_pk_bf16_f32 v181, v186, v187\n\t" \
    "v_exp_f32 v186, v174\n\t" \
    "v_exp_f32 v187, v175\n\t" \
    "v_mfma_f32_32x32x16_bf16 v[160:175], v[208:211], v[72:75], 0\n\t" \
    "v_cvt_pk_bf16_f32 v182, v184, v185\n\t" \
    "v_exp_f32 v184, v144\n\t" \
    "v_exp_f32 v185, v145\n\t" \
    "v_cvt_pk_bf16_f32 v183, v186, v187\n\t" \
    "v_exp_f32 v186, v146\n\t" \
    "v_exp_f32 v187, v147\n\t" \
    "v_mfma_f32_32x32x16_bf16 v[48:63], v[204:207], v[180:183], v[48:63]\n\t" \
    "v_cvt_pk_bf16_f32 v176, v184, v185\n\t" \
    "v_exp_f32 v184, v148\n\t" \
    "v_exp_f32 v185, v149\n\t" \
    "v_cvt_pk_bf16_f32 v177, v186, v187\n\t" \
    "v_exp_f32 v186, v150\n\t" \
    "v_exp_f32 v187, v151\n\t" \
    "v_mfma_f32_32x32x16_bf16 v[160:175], v[212:215], v[76:79], v[160:175]\n\t" \
    "v_cvt_pk_bf16_f32 v178, v184, v185\n\t" \
    "v_exp_f32 v184, v152\n\t" \
    "v_exp_f32 v185, v153\n\t" \
    "v_cvt_pk_bf16_f32 v179, v186, v187\n\t" \
    "v_exp_f32 v186, v154\n\t" \
    "v_exp_f32 v187, v155\n\t" \
    "s_waitcnt vmcnt(10)\n\t" \
    "v_mfma_f32_32x32x16_bf16 v[0:15], v[216:219], v[176:179], v[0:15]\n\t" \
    "v_cvt_pk_bf16_f32 v180, v184, v185\n\t" \
    "v_exp_f32 v184, v156\n\t" \
    "v_exp_f32 v185, v157\n\t" \
    "v_cvt_pk_bf16_f32 v181, v186, v187\n\t" \
    "v_exp_f32 v186, v158\n\t" \
    "v_exp_f32 v187, v159\n\t" \
    "s_setprio 3\n\t" \
    "global_load_dwordx4 v[200:203], %[vo], %[kv] offset:0\n\t" \
    "global_load_dwordx4 v[204:207], %[vo], %[kv] offset:1024\n\t" \
    "s_setprio 0\n\t" \
    "v_mfma_f32_32x32x16_bf16 v[144:159], v[208:211], v[80:83], 0\n\t" \
    "v_cvt_pk_bf16_f32 v182, v184, v185\n\t" \
    "v_exp_f32 v184, v160\n\t" \
    "v_exp_f32 v185, v161\n\t" \
    "v_cvt_pk_bf16_f32 v183, v186, v187\n\t" \
    "v_exp_f32 v186, v162\n\t" \
    "v_exp_f32 v187, v163\n\t" \
    "v_mfma_f32_32x32x16_bf16 v[0:15], v[220:223], v[180:183], v[0:15]\n\t" \
    "v_cvt_pk_bf16_f32 v176, v184, v185\n\t" \
    "v_exp_f32 v184, v164\n\t" \
    "v_exp_f32 v185, v165\n\t" \
    "v_cvt_pk_bf16_f32 v177, v186, v187\n\t" \
    "v_exp_f32 v186, v166\n\t" \
    "v_exp_f32 v187, v167\n\t" \
    "v_mfma_f32_32x32x16_bf16 v[144:159], v[212:215], v[84:87], v[144:159]\n\t" \
    "v_cvt_pk_bf16_f32 v178, v184, v185\n\t" \
    "v_exp_f32 v184, v168\n\t" \
    "v_exp_f32 v185, v169\n\t" \
    "v_cvt_pk_bf16_f32 v179, v186, v187\n\t" \
    "v_exp_f32 v186, v170\n\t" \
    "v_exp_f32 v187, v171\n\t" \
    "v_mfma_f32_32x32x16_bf16 v[16:31], v[216:219], v[176:179], v[16:31]\n\t" \
    "v_cvt_pk_bf16_f32 v180, v184, v185\n\t" \
    "v_exp_f32 v184, v172\n\t" \
    "v_exp_f32 v185, v173\n\t" \
    "v_cvt_pk_bf16_f32 v181, v186, v187\n\t" \
    "v_exp_f32 v186, v174\n\t" \
    "v_exp_f32 v187, v175\n\t" \
    "v_mfma_f32_32x32x16_bf16 v[160:175], v[208:211], v[88:91], 0\n\t" \
    "v_cvt_pk_bf16_f32 v182, v184, v185\n\t" \
    "v_exp_f32 v184, v144\n\t" \
    "v_exp_f32 v185, v145\n\t" \
    "v_cvt_pk_bf16_f32 v183, v186, v187\n\t" \
    "v_exp_f32 v186, v146\n\t" \
    "v_exp_f32 v187, v147\n\t" \
    "v_mfma_f32_32x32x16_bf16 v[16:31], v[220:223], v[180:183], v[16:31]\n\t" \
    "v_cvt_pk_bf16_f32 v176, v184, v185\n\t" \
    "v_exp_f32 v184, v148\n\t" \
    "v_exp_f32 v185, v149\n\t" \
    "v_cvt_pk_bf16_f32 v177, v186, v187\n\t" \
    "v_exp_f32 v186, v150\n\t" \
    "v_exp_f32 v187, v151\n\t" \
    "v_mfma_f32_32x32x16_bf16 v[160:175], v[212:215], v[92:95], v[160:175]\n\t" \
    "v_cvt_pk_bf16_f32 v178, v184, v185\n\t" \
    "v_exp_f32 v184, v152\n\t" \
    "v_exp_f32 v185, v153\n\t" \
    "v_cvt_pk_bf16_f32 v179, v186, v187\n\t" \
    "v_exp_f32 v186, v154\n\t" \
    "v_exp_f32 v187, v155\n\t" \
    "v_mfma_f32_32x32x16_bf16 v[32:47], v[216:219], v[176:179], v[32:47]\n\t" \
    "v_cvt_pk_bf16_f32 v180, v184, v185\n\t" \
    "v_exp_f32 v184, v156\n\t" \
    "v_exp_f32 v185, v157\n\t" \
    "v_cvt_pk_bf16_f32 v181, v186, v187\n\t" \
    "v_exp_f32 v186, v158\n\t" \
    "v_exp_f32 v187, v159\n\t" \
    "s_waitcnt vmcnt(10)\n\t" \
    "s_setprio 3\n\t" \
    "global_load_dwordx4 v[208:211], %[vo], %[kv] offset:2048\n\t" \
    "global_load_dwordx4 v[212:215], %[vo], %[kv] offset:3072\n\t" \
    "s_setprio 0\n\t" \
    "v_add_u32 %[vo], 0x2000, %[vo]\n\t" \
    "v_mfma_f32_32x32x16_bf16 v[144:159], v[224:227], v[64:67], 0\n\t" \
    "v_cvt_pk_bf16_f32 v182, v184, v185\n\t" \
    "v_exp_f32 v184, v160\n\t" \
    "v_exp_f32 v185, v161\n\t" \
    "v_cvt_pk_bf16_f32 v183, v186, v187\n\t" \
    "v_exp_f32 v186, v162\n\t" \
    "v_exp_f32 v187, v163\n\t" \
    "v_mfma_f32_32x32x16_bf16 v[32:47], v[220:223], v[180:183], v[32:47]\n\t" \
    "v_cvt_pk_bf16_f32 v176, v184, v185\n\t" \
    "v_exp_f32 v184, v164\n\t" \
    "v_exp_f32 v185, v165\n\t" \
    "v_cvt_pk_bf16_f32 v177, v186, v187\n\t" \
    "v_exp_f32 v186, v166\n\t" \
    "v_exp_f32 v187, v167\n\t" \
    "v_mfma_f32_32x32x16_bf16 v[144:159], v[228:231], v[68:71], v[144:159]\n\t" \
    "v_cvt_pk_bf16_f32 v178, v184, v185\n\t" \
    "v_exp_f32 v184, v168\n\t" \
    "v_exp_f32 v185, v169\n\t" \
    "v_cvt_pk_bf16_f32 v179, v186, v187\n\t" \
    "v_exp_f32 v186, v170\n\t" \
    "v_exp_f32 v187, v171\n\t" \
    "v_mfma_f32_32x32x16_bf16 v[48:63], v[216:219], v[176:179], v[48:63]\n\t" \
    "v_cvt_pk_bf16_f32 v180, v184, v185\n\t" \
    "v_exp_f32 v184, v172\n\t" \
    "v_exp_f32 v185, v173\n\t" \
    "v_cvt_pk_bf16_f32 v181, v186, v187\n\t" \
    "v_exp_f32 v186, v174\n\t" \
    "v_exp_f32 v187, v175\n\t" \
    "v_mfma_f32_32x32x16_bf16 v[160:175], v[224:227], v[72:75], 0\n\t" \
    "v_cvt_pk_bf16_f32 v182, v184, v185\n\t" \
    "v_exp_f32 v184, v144\n\t" \
    "v_exp_f32 v185, v145\n\t" \
    "v_cvt_pk_bf16_f32 v183, v186, v187\n\t" \
    "v_exp_f32 v186, v146\n\t" \
    "v_exp_f32 v187, v147\n\t" \
    "v_mfma_f32_32x32x16_bf16 v[48:63], v[220:223], v[180:183], v[48:63]\n\t" \
    "v_cvt_pk_bf16_f32 v176, v184, v185\n\t" \
    "v_exp_f32 v184, v148\n\t" \
    "v_exp_f32 v185, v149\n\t" \
    "v_cvt_pk_bf16_f32 v177, v186, v187\n\t" \
    "v_exp_f32 v186, v150\n\t" \
    "v_exp_f32 v187, v151\n\t" \
    "v_mfma_f32_32x32x16_bf16 v[160:175], v[228:231], v[76:79], v[160:175]\n\t" \
    "v_cvt_pk_bf16_f32 v178, v184, v185\n\t" \
    "v_exp_f32 v184, v152\n\t" \
    "v_exp_f32 v185, v153\n\t" \
    "v_cvt_pk_bf16_f32 v179, v186, v187\n\t" \
    "v_exp_f32 v186, v154\n\t" \
    "v_exp_f32 v187, v155\n\t" \
    "s_waitcnt vmcnt(10)\n\t" \
    "v_mfma_f32_32x32x16_bf16 v[0:15], v[232:235], v[176:179], v[0:15]\n\t" \
    "v_cvt_pk_bf16_f32 v180, v184, v185\n\t" \
    "v_exp_f32 v184, v156\n\t" \
    "v_exp_f32 v185, v157\n\t" \
    "v_cvt_pk_bf16_f32 v181, v186, v187\n\t" \
    "v_exp_f32 v186, v158\n\t" \
    "v_exp_f32 v187, v159\n\t" \
    "s_setprio 3\n\t" \
    "global_load_dwordx4 v[216:219], %[vo], %[kv] offset:-4096\n\t" \
    "global_load_dwordx4 v[220:223], %[vo], %[kv] offset:-3072\n\t" \
    "s_setprio 0\n\t" \
    "v_mfma_f32_32x32x16_bf16 v[144:159], v[224:227], v[80:83], 0\n\t" \
    "v_cvt_pk_bf16_f32 v182, v184, v185\n\t" \
    "v_exp_f32 v184, v160\n\t" \
    "v_exp_f32 v185, v161\n\t" \
    "v_cvt_pk_bf16_f32 v183, v186, v187\n\t" \
    "v_exp_f32 v186, v162\n\t" \
    "v_exp_f32 v187, v163\n\t" \
    "v_mfma_f32_32x32x16_bf16 v[0:15], v[236:239], v[180:183], v[0:15]\n\t" \
    "v_cvt_pk_bf16_f32 v176, v184, v185\n\t" \
    "v_exp_f32 v184, v164\n\t" \
    "v_exp_f32 v185, v165\n\t" \
    "v_cvt_pk_bf16_f32 v177, v186, v187\n\t" \
    "v_exp_f32 v186, v166\n\t" \
    "v_exp_f32 v187, v167\n\t" \
    "v_mfma_f32_32x32x16_bf16 v[144:159], v[228:231], v[84:87], v[144:159]\n\t" \
    "v_cvt_pk_bf16_f32 v178, v184, v185\n\t" \
    "v_exp_f32 v184, v168\n\t" \
    "v_exp_f32 v185, v169\n\t" \
    "v_cvt_pk_bf16_f32 v179, v186, v187\n\t" \
    "v_exp_f32 v186, v170\n\t" \
    "v_exp_f32 v187, v171\n\t" \
    "v_mfma_f32_32x32x16_bf16 v[16:31], v[232:235], v[176:179], v[16:31]\n\t" \
    "v_cvt_pk_bf16_f32 v180, v184, v185\n\t" \
    "v_exp_f32 v184, v172\n\t" \
    "v_exp_f32 v185, v173\n\t" \
    "v_cvt_pk_bf16_f32 v181, v186, v187\n\t" \
    "v_exp_f32 v186, v174\n\t" \
    "v_exp_f32 v187, v175\n\t" \
    "v_mfma_f32_32x32x16_bf16 v[160:175], v[224:227], v[88:91], 0\n\t" \
    "v_cvt_pk_bf16_f32 v182, v184, v185\n\t" \
    "v_exp_f32 v184, v144\n\t" \
    "v_exp_f32 v185, v145\n\t" \
    "v_cvt_pk_bf16_f32 v183, v186, v187\n\t" \
    "v_exp_f32 v186, v146\n\t" \
    "v_exp_f32 v187, v147\n\t" \
    "v_mfma_f32_32x32x16_bf16 v[16:31], v[236:239], v[180:183], v[16:31]\n\t" \
    "v_cvt_pk_bf16_f32 v176, v184, v185\n\t" \
    "v_exp_f32 v184, v148\n\t" \
    "v_exp_f32 v185, v149\n\t" \
    "v_cvt_pk_bf16_f32 v177, v186, v187\n\t" \
    "v_exp_f32 v186, v150\n\t" \
    "v_exp_f32 v187, v151\n\t" \
    "v_mfma_f32_32x32x16_bf16 v[160:175], v[228:231], v[92:95], v[160:175]\n\t" \
    "v_cvt_pk_bf16_f32 v178, v184, v185\n\t" \
    "v_exp_f32 v184, v152\n\t" \
    "v_exp_f32 v185, v153\n\t" \
    "v_cvt_pk_bf16_f32 v179, v186, v187\n\t" \
    "v_exp_f32 v186, v154\n\t" \
    "v_exp_f32 v187, v155\n\t" \
    "v_mfma_f32_32x32x16_bf16 v[32:47], v[232:235], v[176:179], v[32:47]\n\t" \
    "v_cvt_pk_bf16_f32 v180, v184, v185\n\t" \
    "v_exp_f32 v184, v156\n\t" \
    "v_exp_f32 v185, v157\n\t" \
    "v_cvt_pk_bf16_f32 v181, v186, v187\n\t" \
    "v_exp_f32 v186, v158\n\t" \
    "v_exp_f32 v187, v159\n\t" \
    "s_waitcnt vmcnt(10)\n\t" \
    "s_setprio 3\n\t" \
    "global_load_dwordx4 v[224:227], %[vo], %[kv] offset:-2048\n\t" \
    "global_load_dwordx4 v[228:231], %[vo], %[kv] offset:-1024\n\t" \
    "s_setprio 0\n\t" \
    "v_mfma_f32_32x32x16_bf16 v[144:159], v[240:243], v[64:67], 0\n\t" \
    "v_cvt_pk_bf16_f32 v182, v184, v185\n\t" \
    "v_exp_f32 v184, v160\n\t" \
    "v_exp_f32 v185, v161\n\t" \
    "v_cvt_pk_bf16_f32 v183, v186, v187\n\t" \
    "v_exp_f32 v186, v162\n\t" \
    "v_exp_f32 v187, v163\n\t" \
    "v_mfma_f32_32x32x16_bf16 v[32:47], v[236:239], v[180:183], v[32:47]\n\t" \
    "v_cvt_pk_bf16_f32 v176, v184, v185\n\t" \
    "v_exp_f32 v184, v164\n\t" \
    "v_exp_f32 v185, v165\n\t" \
    "v_cvt_pk_bf16_f32 v177, v186, v187\n\t" \
    "v_exp_f32 v186, v166\n\t" \
    "v_exp_f32 v187, v167\n\t" \
    "v_mfma_f32_32x32x16_bf16 v[144:159], v[244:247], v[68:71], v[144:159]\n\t" \
    "v_cvt_pk_bf16_f32 v178, v184, v185\n\t" \
    "v_exp_f32 v184, v168\n\t" \
    "v_exp_f32 v185, v169\n\t" \
    "v_cvt_pk_bf16_f32 v179, v186, v187\n\t" \
    "v_exp_f32 v186, v170\n\t" \
    "v_exp_f32 v187, v171\n\t" \
    "v_mfma_f32_32x32x16_bf16 v[48:63], v[232:235], v[176:179], v[48:63]\n\t" \
    "v_cvt_pk_bf16_f32 v180, v184, v185\n\t" \
    "v_exp_f32 v184, v172\n\t" \
    "v_exp_f32 v185, v173\n\t" \
    "v_cvt_pk_bf16_f32 v181, v186, v187\n\t" \
    "v_exp_f32 v186, v174\n\t" \
    "v_exp_f32 v187, v175\n\t" \
    "v_mfma_f32_32x32x16_bf16 v[160:175], v[240:243], v[72:75], 0\n\t" \
    "v_cvt_pk_bf16_f32 v182, v184, v185\n\t" \
    "v_exp_f32 v184, v144\n\t" \
    "v_exp_f32 v185, v145\n\t" \
    "v_cvt_pk_bf16_f32 v183, v186, v187\n\t" \
    "v_exp_f32 v186, v146\n\t" \
    "v_exp_f32 v187, v147\n\t" \
    "v_mfma_f32_32x32x16_bf16 v[48:63], v[236:239], v[180:183], v[48:63]\n\t" \
    "v_cvt_pk_bf16_f32 v176, v184, v185\n\t" \
    "v_exp_f32 v184, v148\n\t" \
    "v_exp_f32 v185, v149\n\t" \
    "v_cvt_pk_bf16_f32 v177, v186, v187\n\t" \
    "v_exp_f32 v186, v150\n\t" \
    "v_exp_f32 v187, v151\n\t" \
    "v_mfma_f32_32x32x16_bf16 v[160:175], v[244:247], v[76:79], v[160:175]\n\t" \
    "v_cvt_pk_bf16_f32 v178, v184, v185\n\t" \
    "v_exp_f32 v184, v152\n\t" \
    "v_exp_f32 v185, v153\n\t" \
    "v_cvt_pk_bf16_f32 v179, v186, v187\n\t" \
    "v_exp_f32 v186, v154\n\t" \
    "v_exp_f32 v187, v155\n\t" \
    "s_waitcnt vmcnt(10)\n\t" \
    "v_mfma_f32_32x32x16_bf16 v[0:15], v[248:251], v[176:179], v[0:15]\n\t" \
    "v_cvt_pk_bf16_f32 v180, v184, v185\n\t" \
    "v_exp_f32 v184, v156\n\t" \
    "v_exp_f32 v185, v157\n\t" \
    "v_cvt_pk_bf16_f32 v181, v186, v187\n\t" \
    "v_exp_f32 v186, v158\n\t" \
    "v_exp_f32 v187, v159\n\t" \
    "s_setprio 3\n\t" \
    "global_load_dwordx4 v[232:235], %[vo], %[kv] offset:0\n\t" \
    "global_load_dwordx4 v[236:239], %[vo], %[kv] offset:1024\n\t" \
    "s_setprio 0\n\t" \
    "v_mfma_f32_32x32x16_bf16 v[144:159], v[240:243], v[80:83], 0\n\t" \
    "v_cvt_pk_bf16_f32 v182, v184, v185\n\t" \
    "v_exp_f32 v184, v160\n\t" \
    "v_exp_f32 v185, v161\n\t" \
    "v_cvt_pk_bf16_f32 v183, v186, v187\n\t" \
    "v_exp_f32 v186, v162\n\t" \
    "v_exp_f32 v187, v163\n\t" \
    "v_mfma_f32_32x32x16_bf16 v[0:15], v[252:255], v[180:183], v[0:15]\n\t" \
    "v_cvt_pk_bf16_f32 v176, v184, v185\n\t" \
    "v_exp_f32 v184, v164\n\t" \
    "v_exp_f32 v185, v165\n\t" \
    "v_cvt_pk_bf16_f32 v177, v186, v187\n\t" \
    "v_exp_f32 v186, v166\n\t" \
    "v_exp_f32 v187, v167\n\t" \
    "v_mfma_f32_32x32x16_bf16 v[144:159], v[244:247], v[84:87], v[144:159]\n\t" \
    "v_cvt_pk_bf16_f32 v178, v184, v185\n\t" \
    "v_exp_f32 v184, v168\n\t" \
    "v_exp_f32 v185, v169\n\t" \
    "v_cvt_pk_bf16_f32 v179, v186, v187\n\t" \
    "v_exp_f32 v186, v170\n\t" \
    "v_exp_f32 v187, v171\n\t" \
    "v_mfma_f32_32x32x16_bf16 v[16:31], v[248:251], v[176:179], v[16:31]\n\t" \
    "v_cvt_pk_bf16_f32 v180, v184, v185\n\t" \
    "v_exp_f32 v184, v172\n\t" \
    "v_exp_f32 v185, v173\n\t" \
    "v_cvt_pk_bf16_f32 v181, v186, v187\n\t" \
    "v_exp_f32 v186, v174\n\t" \
    "v_exp_f32 v187, v175\n\t" \
    "v_mfma_f32_32x32x16_bf16 v[160:175], v[240:243], v[88:91], 0\n\t" \
    "v_cvt_pk_bf16_f32 v182, v184, v185\n\t" \
    "v_exp_f32 v184, v144\n\t" \
    "v_exp_f32 v185, v145\n\t" \
    "v_cvt_pk_bf16_f32 v183, v186, v187\n\t" \
    "v_exp_f32 v186, v146\n\t" \
    "v_exp_f32 v187, v147\n\t" \
    "v_mfma_f32_32x32x16_bf16 v[16:31], v[252:255], v[180:183], v[16:31]\n\t" \
    "v_cvt_pk_bf16_f32 v176, v184, v185\n\t" \
    "v_exp_f32 v184, v148\n\t" \
    "v_exp_f32 v185, v149\n\t" \
    "v_cvt_pk_bf16_f32 v177, v186, v187\n\t" \
    "v_exp_f32 v186, v150\n\t" \
    "v_exp_f32 v187, v151\n\t" \
    "v_mfma_f32_32x32x16_bf16 v[160:175], v[244:247], v[92:95], v[160:175]\n\t" \
    "v_cvt_pk_bf16_f32 v178, v184, v185\n\t" \
    "v_exp_f32 v184, v152\n\t" \
    "v_exp_f32 v185, v153\n\t" \
    "v_cvt_pk_bf16_f32 v179, v186, v187\n\t" \
    "v_exp_f32 v186, v154\n\t" \
    "v_exp_f32 v187, v155\n\t" \
    "v_mfma_f32_32x32x16_bf16 v[32:47], v[248:251], v[176:179], v[32:47]\n\t" \
    "v_cvt_pk_bf16_f32 v180, v184, v185\n\t" \
    "v_exp_f32 v184, v156\n\t" \
    "v_exp_f32 v185, v157\n\t" \
    "v_cvt_pk_bf16_f32 v181, v186, v187\n\t" \
    "v_exp_f32 v186, v158\n\t" \
    "v_exp_f32 v187, v159\n\t" \
    "s_waitcnt vmcnt(10)\n\t" \
    "s_setprio 3\n\t" \
    "global_load_dwordx4 v[240:243], %[vo], %[kv] offset:2048\n\t" \
    "global_load_dwordx4 v[244:247], %[vo], %[kv] offset:3072\n\t" \
    "s_setprio 0\n\t" \
    "v_add_u32 %[vo], 0x2000, %[vo]\n\t" \
    "v_mfma_f32_32x32x16_bf16 v[144:159], v[192:195], v[64:67], 0\n\t" \
    "v_cvt_pk_bf16_f32 v182, v184, v185\n\t" \
    "v_exp_f32 v184, v160\n\t" \
    "v_exp_f32 v185, v161\n\t" \
    "v_cvt_pk_bf16_f32 v183, v186, v187\n\t" \
    "v_exp_f32 v186, v162\n\t" \
    "v_exp_f32 v187, v163\n\t" \
    "v_mfma_f32_32x32x16_bf16 v[32:47], v[252:255], v[180:183], v[32:47]\n\t" \
    "v_cvt_pk_bf16_f32 v176, v184, v185\n\t" \
    "v_exp_f32 v184, v164\n\t" \
    "v_exp_f32 v185, v165\n\t" \
    "v_cvt_pk_bf16_f32 v177, v186, v187\n\t" \
    "v_exp_f32 v186, v166\n\t" \
    "v_exp_f32 v187, v167\n\t" \
    "v_mfma_f32_32x32x16_bf16 v[144:159], v[196:199], v[68:71], v[144:159]\n\t" \
    "v_cvt_pk_bf16_f32 v178, v184, v185\n\t" \
    "v_exp_f32 v184, v168\n\t" \
    "v_exp_f32 v185, v169\n\t" \
    "v_cvt_pk_bf16_f32 v179, v186, v187\n\t" \
    "v_exp_f32 v186, v170\n\t" \
    "v_exp_f32 v187, v171\n\t" \
    "v_mfma_f32_32x32x16_bf16 v[48:63], v[248:251], v[176:179], v[48:63]\n\t" \
    "v_cvt_pk_bf16_f32 v180, v184, v185\n\t" \
    "v_exp_f32 v184, v172\n\t" \
    "v_exp_f32 v185, v173\n\t" \
    "v_cvt_pk_bf16_f32 v181, v186, v187\n\t" \
    "v_exp_f32 v186, v174\n\t" \
    "v_exp_f32 v187, v175\n\t" \
    "s_sub_u32 %[cnt], %[cnt], 1\n\t" \
    "s_cmp_lg_u32 %[cnt], 0\n\t" \
    "s_cbranch_scc1 1b\n\t" \
    "v_mfma_f32_32x32x16_bf16 v[160:175], v[192:195], v[72:75], 0\n\t" \
    "v_cvt_pk_bf16_f32 v182, v184, v185\n\t" \
    "v_exp_f32 v184, v144\n\t" \
    "v_exp_f32 v185, v145\n\t" \
    "v_cvt_pk_bf16_f32 v183, v186, v187\n\t" \
    "v_exp_f32 v186, v146\n\t" \
    "v_exp_f32 v187, v147\n\t" \
    "v_mfma_f32_32x32x16_bf16 v[48:63], v[252:255], v[180:183], v[48:63]\n\t" \
    "v_cvt_pk_bf16_f32 v176, v184, v185\n\t" \
    "v_exp_f32 v184, v148\n\t" \
    "v_exp_f32 v185, v149\n\t" \
    "v_cvt_pk_bf16_f32 v177, v186, v187\n\t" \
    "v_exp_f32 v186, v150\n\t" \
    "v_exp_f32 v187, v151\n\t" \
    "v_mfma_f32_32x32x16_bf16 v[160:175], v[196:199], v[76:79], v[160:175]\n\t" \
    "v_cvt_pk_bf16_f32 v178, v184, v185\n\t" \
    "v_exp_f32 v184, v152\n\t" \
    "v_exp_f32 v185, v153\n\t" \
    "v_cvt_pk_bf16_f32 v179, v186, v187\n\t" \
    "v_exp_f32 v186, v154\n\t" \
    "v_exp_f32 v187, v155\n\t" \
    "s_waitcnt vmcnt(10)\n\t" \
    "v_mfma_f32_32x32x16_bf16 v[0:15], v[200:203], v[176:179], v[0:15]\n\t" \
    "v_cvt_pk_bf16_f32 v180, v184, v185\n\t" \
    "v_exp_f32 v184, v156\n\t" \
    "v_exp_f32 v185, v157\n\t" \
    "v_cvt_pk_bf16_f32 v181, v186, v187\n\t" \
    "v_exp_f32 v186, v158\n\t" \
    "v_exp_f32 v187, v159\n\t" \
    "s_setprio 3\n\t" \
    "global_load_dwordx4 v[248:251], %[vo], %[kv] offset:-4096\n\t" \
    "global_load_dwordx4 v[252:255], %[vo], %[kv] offset:-3072\n\t" \
    "s_setprio 0\n\t" \
    "v_mfma_f32_32x32x16_bf16 v[144:159], v[192:195], v[80:83], 0\n\t" \
    "v_cvt_pk_bf16_f32 v182, v184, v185\n\t" \
    "v_exp_f32 v184, v160\n\t" \
    "v_exp_f32 v185, v161\n\t" \
    "v_cvt_pk_bf16_f32 v183, v186, v187\n\t" \
    "v_exp_f32 v186, v162\n\t" \
    "v_exp_f32 v187, v163\n\t" \
    "v_mfma_f32_32x32x16_bf16 v[0:15], v[204:207], v[180:183], v[0:15]\n\t" \
    "v_cvt_pk_bf16_f32 v176, v184, v185\n\t" \
    "v_exp_f32 v184, v164\n\t" \
    "v_exp_f32 v185, v165\n\t" \
    "v_cvt_pk_bf16_f32 v177, v186, v187\n\t" \
    "v_exp_f32 v186, v166\n\t" \
    "v_exp_f32 v187, v167\n\t" \
    "v_mfma_f32_32x32x16_bf16 v[144:159], v[196:199], v[84:87], v[144:159]\n\t" \
    "v_cvt_pk_bf16_f32 v178, v184, v185\n\t" \
    "v_exp_f32 v184, v168\n\t" \
    "v_exp_f32 v185, v169\n\t" \
    "v_cvt_pk_bf16_f32 v179, v186, v187\n\t" \
    "v_exp_f32 v186, v170\n\t" \
    "v_exp_f32 v187, v171\n\t" \
    "v_mfma_f32_32x32x16_bf16 v[16:31], v[200:203], v[176:179], v[16:31]\n\t" \
    "v_cvt_pk_bf16_f32 v180, v184, v185\n\t" \
    "v_exp_f32 v184, v172\n\t" \
    "v_exp_f32 v185, v173\n\t" \
    "v_cvt_pk_bf16_f32 v181, v186, v187\n\t" \
    "v_exp_f32 v186, v174\n\t" \
    "v_exp_f32 v187, v175\n\t" \
    "v_mfma_f32_32x32x16_bf16 v[160:175], v[192:195], v[88:91], 0\n\t" \
    "v_cvt_pk_bf16_f32 v182, v184, v185\n\t" \
    "v_exp_f32 v184, v144\n\t" \
    "v_exp_f32 v185, v145\n\t" \
    "v_cvt_pk_bf16_f32 v183, v186, v187\n\t" \
    "v_exp_f32 v186, v146\n\t" \
    "v_exp_f32 v187, v147\n\t" \
    "v_mfma_f32_32x32x16_bf16 v[16:31], v[204:207], v[180:183], v[16:31]\n\t" \
    "v_cvt_pk_bf16_f32 v176, v184, v185\n\t" \
    "v_exp_f32 v184, v148\n\t" \
    "v_exp_f32 v185, v149\n\t" \
    "v_cvt_pk_bf16_f32 v177, v186, v187\n\t" \
    "v_exp_f32 v186, v150\n\t" \
    "v_exp_f32 v187, v151\n\t" \
    "v_mfma_f32_32x32x16_bf16 v[160:175], v[196:199], v[92:95], v[160:175]\n\t" \
    "v_cvt_pk_bf16_f32 v178, v184, v185\n\t" \
    "v_exp_f32 v184, v152\n\t" \
    "v_exp_f32 v185, v153\n\t" \
    "v_cvt_pk_bf16_f32 v179, v186, v187\n\t" \
    "v_exp_f32 v186, v154\n\t" \
    "v_exp_f32 v187, v155\n\t" \
    "v_mfma_f32_32x32x16_bf16 v[32:47], v[200:203], v[176:179], v[32:47]\n\t" \
    "v_cvt_pk_bf16_f32 v180, v184, v185\n\t" \
    "v_exp_f32 v184, v156\n\t" \
    "v_exp_f32 v185, v157\n\t" \
    "v_cvt_pk_bf16_f32 v181, v186, v187\n\t" \
    "v_exp_f32 v186, v158\n\t" \
    "v_exp_f32 v187, v159\n\t" \
    "s_waitcnt vmcnt(10)\n\t" \
    "global_load_dwordx4 v[192:195], %[lo], s[48:49] offset:-4096\n\t" \
    "global_load_dwordx4 v[196:199], %[lo], s[50:51] offset:-4096\n\t" \
    "v_mfma_f32_32x32x16_bf16 v[144:159], v[208:211], v[64:67], 0\n\t" \
    "v_cvt_pk_bf16_f32 v182, v184, v185\n\t" \
    "v_exp_f32 v184, v160\n\t" \
    "v_exp_f32 v185, v161\n\t" \
    "v_cvt_pk_bf16_f32 v183, v186, v187\n\t" \
    "v_exp_f32 v186, v162\n\t" \
    "v_exp_f32 v187, v163\n\t" \
    "v_mfma_f32_32x32x16_bf16 v[32:47], v[204:207], v[180:183], v[32:47]\n\t" \
    "v_cvt_pk_bf16_f32 v176, v184, v185\n\t" \
    "v_exp_f32 v184, v164\n\t" \
    "v_exp_f32 v185, v165\n\t" \
    "v_cvt_pk_bf16_f32 v177, v186, v187\n\t" \
    "v_exp_f32 v186, v166\n\t" \
    "v_exp_f32 v187, v167\n\t" \
    "v_mfma_f32_32x32x16_bf16 v[144:159], v[212:215], v[68:71], v[144:159]\n\t" \
    "v_cvt_pk_bf16_f32 v178, v184, v185\n\t" \
    "v_exp_f32 v184, v168\n\t" \
    "v_exp_f32 v185, v169\n\t" \
    "v_cvt_pk_bf16_f32 v179, v186, v187\n\t" \
    "v_exp_f32 v186, v170\n\t" \
    "v_exp_f32 v187, v171\n\t" \
    "v_mfma_f32_32x32x16_bf16 v[48:63], v[200:203], v[176:179], v[48:63]\n\t" \
    "v_cvt_pk_bf16_f32 v180, v184, v185\n\t" \
    "v_exp_f32 v184, v172\n\t" \
    "v_exp_f32 v185, v173\n\t" \
    "v_cvt_pk_bf16_f32 v181, v186, v187\n\t" \
    "v_exp_f32 v186, v174\n\t" \
    "v_exp_f32 v187, v175\n\t" \
    "v_mfma_f32_32x32x16_bf16 v[160:175], v[208:211], v[72:75], 0\n\t" \
    "v_cvt_pk_bf16_f32 v182, v184, v185\n\t" \
    "v_exp_f32 v184, v144\n\t" \
    "v_exp_f32 v185, v145\n\t" \
    "v_cvt_pk_bf16_f32 v183, v186, v187\n\t" \
    "v_exp_f32 v186, v146\n\t" \
    "v_exp_f32 v187, v147\n\t" \
    "v_mfma_f32_32x32x16_bf16 v[48:63], v[204:207], v[180:183], v[48:63]\n\t" \
    "v_cvt_pk_bf16_f32 v176, v184, v185\n\t" \
    "v_exp_f32 v184, v148\n\t" \
    "v_exp_f32 v185, v149\n\t" \
    "v_cvt_pk_bf16_f32 v177, v186, v187\n\t" \
    "v_exp_f32 v186, v150\n\t" \
    "v_exp_f32 v187, v151\n\t" \
    "v_mfma_f32_32x32x16_bf16 v[160:175], v[212:215], v[76:79], v[160:175]\n\t" \
    "v_cvt_pk_bf16_f32 v178, v184, v185\n\t" \
    "v_exp_f32 v184, v152\n\t" \
    "v_exp_f32 v185, v153\n\t" \
    "v_cvt_pk_bf16_f32 v179, v186, v187\n\t" \
    "v_exp_f32 v186, v154\n\t" \
    "v_exp_f32 v187, v155\n\t" \
    "s_waitcnt vmcnt(10)\n\t" \
    "v_mfma_f32_32x32x16_bf16 v[0:15], v[216:219], v[176:179], v[0:15]\n\t" \
    "v_cvt_pk_bf16_f32 v180, v184, v185\n\t" \
    "v_exp_f32 v184, v156\n\t" \
    "v_exp_f32 v185, v157\n\t" \
    "v_cvt_pk_bf16_f32 v181, v186, v187\n\t" \
    "v_exp_f32 v186, v158\n\t" \
    "v_exp_f32 v187, v159\n\t" \
    "global_load_dwordx4 v[200:203], %[lo], s[48:49] offset:-3072\n\t" \
    "global_load_dwordx4 v[204:207], %[lo], s[50:51] offset:-3072\n\t" \
    "v_mfma_f32_32x32x16_bf16 v[144:159], v[208:211], v[80:83], 0\n\t" \
    "v_cvt_pk_bf16_f32 v182, v184, v185\n\t" \
    "v_exp_f32 v184, v160\n\t" \
    "v_exp_f32 v185, v161\n\t" \
    "v_cvt_pk_bf16_f32 v183, v186, v187\n\t" \
    "v_exp_f32 v186, v162\n\t" \
    "v_exp_f32 v187, v163\n\t" \
    "v_mfma_f32_32x32x16_bf16 v[0:15], v[220:223], v[180:183], v[0:15]\n\t" \
    "v_cvt_pk_bf16_f32 v176, v184, v185\n\t" \
    "v_exp_f32 v184, v164\n\t" \
    "v_exp_f32 v185, v165\n\t" \
    "v_cvt_pk_bf16_f32 v177, v186, v187\n\t" \
    "v_exp_f32 v186, v166\n\t" \
    "v_exp_f32 v187, v167\n\t" \
    "v_mfma_f32_32x32x16_bf16 v[144:159], v[212:215], v[84:87], v[144:159]\n\t" \
    "v_cvt_pk_bf16_f32 v178, v184, v185\n\t" \
    "v_exp_f32 v184, v168\n\t" \
    "v_exp_f32 v185, v169\n\t" \
    "v_cvt_pk_bf16_f32 v179, v186, v187\n\t" \
    "v_exp_f32 v186, v170\n\t" \
    "v_exp_f32 v187, v171\n\t" \
    "v_mfma_f32_32x32x16_bf16 v[16:31], v[216:219], v[176:179], v[16:31]\n\t" \
    "v_cvt_pk_bf16_f32 v180, v184, v185\n\t" \
    "v_exp_f32 v184, v172\n\t" \
    "v_exp_f32 v185, v173\n\t" \
    "v_cvt_pk_bf16_f32 v181, v186, v187\n\t" \
    "v_exp_f32 v186, v174\n\t" \
    "v_exp_f32 v187, v175\n\t" \
    "v_mfma_f32_32x32x16_bf16 v[160:175], v[208:211], v[88:91], 0\n\t" \
    "v_cvt_pk_bf16_f32 v182, v184, v185\n\t" \
    "v_exp_f32 v184, v144\n\t" \
    "v_exp_f32 v185, v145\n\t" \
    "v_cvt_pk_bf16_f32 v183, v186, v187\n\t" \
    "v_exp_f32 v186, v146\n\t" \
    "v_exp_f32 v187, v147\n\t" \
    "v_mfma_f32_32x32x16_bf16 v[16:31], v[220:223], v[180:183], v[16:31]\n\t" \
    "v_cvt_pk_bf16_f32 v176, v184, v185\n\t" \
    "v_exp_f32 v184, v148\n\t" \
    "v_exp_f32 v185, v149\n\t" \
    "v_cvt_pk_bf16_f32 v177, v186, v187\n\t" \
    "v_exp_f32 v186, v150\n\t" \
    "v_exp_f32 v187, v151\n\t" \
    "v_mfma_f32_32x32x16_bf16 v[160:175], v[212:215], v[92:95], v[160:175]\n\t" \
    "v_cvt_pk_bf16_f32 v178, v184, v185\n\t" \
    "v_exp_f32 v184, v152\n\t" \
    "v_exp_f32 v185, v153\n\t" \
    "v_cvt_pk_bf16_f32 v179, v186, v187\n\t" \
    "v_exp_f32 v186, v154\n\t" \
    "v_exp_f32 v187, v155\n\t" \
    "v_mfma_f32_32x32x16_bf16 v[32:47], v[216:219], v[176:179], v[32:47]\n\t" \
    "v_cvt_pk_bf16_f32 v180, v184, v185\n\t" \
    "v_exp_f32 v184, v156\n\t" \
    "v_exp_f32 v185, v157\n\t" \
    "v_cvt_pk_bf16_f32 v181, v186, v187\n\t" \
    "v_exp_f32 v186, v158\n\t" \
    "v_exp_f32 v187, v159\n\t" \
    "s_waitcnt vmcnt(10)\n\t" \
    "global_load_dwordx4 v[208:211], %[lo], s[48:49] offset:-2048\n\t" \
    "global_load_dwordx4 v[212:215], %[lo], s[50:51] offset:-2048\n\t" \
    "v_mfma_f32_32x32x16_bf16 v[144:159], v[224:227], v[64:67], 0\n\t" \
    "v_cvt_pk_bf16_f32 v182, v184, v185\n\t" \
    "v_exp_f32 v184, v160\n\t" \
    "v_exp_f32 v185, v161\n\t" \
    "v_cvt_pk_bf16_f32 v183, v186, v187\n\t" \
    "v_exp_f32 v186, v162\n\t" \
    "v_exp_f32 v187, v163\n\t" \
    "v_mfma_f32_32x32x16_bf16 v[32:47], v[220:223], v[180:183], v[32:47]\n\t" \
    "v_cvt_pk_bf16_f32 v176, v184, v185\n\t" \
    "v_exp_f32 v184, v164\n\t" \
    "v_exp_f32 v185, v165\n\t" \
    "v_cvt_pk_bf16_f32 v177, v186, v187\n\t" \
    "v_exp_f32 v186, v166\n\t" \
    "v_exp_f32 v187, v167\n\t" \
    "v_mfma_f32_32x32x16_bf16 v[144:159], v[228:231], v[68:71], v[144:159]\n\t" \
    "v_cvt_pk_bf16_f32 v178, v184, v185\n\t" \
    "v_exp_f32 v184, v168\n\t" \
    "v_exp_f32 v185, v169\n\t" \
    "v_cvt_pk_bf16_f32 v179, v186, v187\n\t" \
    "v_exp_f32 v186, v170\n\t" \
    "v_exp_f32 v187, v171\n\t" \
    "v_mfma_f32_32x32x16_bf16 v[48:63], v[216:219], v[176:179], v[48:63]\n\t" \
    "v_cvt_pk_bf16_f32 v180, v184, v185\n\t" \
    "v_exp_f32 v184, v172\n\t" \
    "v_exp_f32 v185, v173\n\t" \
    "v_cvt_pk_bf16_f32 v181, v186, v187\n\t" \
    "v_exp_f32 v186, v174\n\t" \
    "v_exp_f32 v187, v175\n\t" \
    "v_mfma_f32_32x32x16_bf16 v[160:175], v[224:227], v[72:75], 0\n\t" \
    "v_cvt_pk_bf16_f32 v182, v184, v185\n\t" \
    "v_exp_f32 v184, v144\n\t" \
    "v_exp_f32 v185, v145\n\t" \
    "v_cvt_pk_bf16_f32 v183, v186, v187\n\t" \
    "v_exp_f32 v186, v146\n\t" \
    "v_exp_f32 v187, v147\n\t" \
    "v_mfma_f32_32x32x16_bf16 v[48:63], v[220:223], v[180:183], v[48:63]\n\t" \
    "v_cvt_pk_bf16_f32 v176, v184, v185\n\t" \
    "v_exp_f32 v184, v148\n\t" \
    "v_exp_f32 v185, v149\n\t" \
    "v_cvt_pk_bf16_f32 v177, v186, v187\n\t" \
    "v_exp_f32 v186, v150\n\t" \
    "v_exp_f32 v187, v151\n\t" \
    "v_mfma_f32_32x32x16_bf16 v[160:175], v[228:231], v[76:79], v[160:175]\n\t" \
    "v_cvt_pk_bf16_f32 v178, v184, v185\n\t" \
    "v_exp_f32 v184, v152\n\t" \
    "v_exp_f32 v185, v153\n\t" \
    "v_cvt_pk_bf16_f32 v179, v186, v187\n\t" \
    "v_exp_f32 v186, v154\n\t" \
    "v_exp_f32 v187, v155\n\t" \
    "s_waitcnt vmcnt(10)\n\t" \
    "v_mfma_f32_32x32x16_bf16 v[0:15], v[232:235], v[176:179], v[0:15]\n\t" \
    "v_cvt_pk_bf16_f32 v180, v184, v185\n\t" \
    "v_exp_f32 v184, v156\n\t" \
    "v_exp_f32 v185, v157\n\t" \
    "v_cvt_pk_bf16_f32 v181, v186, v187\n\t" \
    "v_exp_f32 v186, v158\n\t" \
    "v_exp_f32 v187, v159\n\t" \
    "global_load_dwordx4 v[216:219], %[lo], s[48:49] offset:-1024\n\t" \
    "global_load_dwordx4 v[220:223], %[lo], s[50:51] offset:-1024\n\t" \
    "v_mfma_f32_32x32x16_bf16 v[144:159], v[224:227], v[80:83], 0\n\t" \
    "v_cvt_pk_bf16_f32 v182, v184, v185\n\t" \
    "v_exp_f32 v184, v160\n\t" \
    "v_exp_f32 v185, v161\n\t" \
    "v_cvt_pk_bf16_f32 v183, v186, v187\n\t" \
    "v_exp_f32 v186, v162\n\t" \
    "v_exp_f32 v187, v163\n\t" \
    "v_mfma_f32_32x32x16_bf16 v[0:15], v[236:239], v[180:183], v[0:15]\n\t" \
    "v_cvt_pk_bf16_f32 v176, v184, v185\n\t" \
    "v_exp_f32 v184, v164\n\t" \
    "v_exp_f32 v185, v165\n\t" \
    "v_cvt_pk_bf16_f32 v177, v186, v187\n\t" \
    "v_exp_f32 v186, v166\n\t" \
    "v_exp_f32 v187, v167\n\t" \
    "v_mfma_f32_32x32x16_bf16 v[144:159], v[228:231], v[84:87], v[144:159]\n\t" \
    "v_cvt_pk_bf16_f32 v178, v184, v185\n\t" \
    "v_exp_f32 v184, v168\n\t" \
    "v_exp_f32 v185, v169\n\t" \
    "v_cvt_pk_bf16_f32 v179, v186, v187\n\t" \
    "v_exp_f32 v186, v170\n\t" \
    "v_exp_f32 v187, v171\n\t" \
    "v_mfma_f32_32x32x16_bf16 v[16:31], v[232:235], v[176:179], v[16:31]\n\t" \
    "v_cvt_pk_bf16_f32 v180, v184, v185\n\t" \
    "v_exp_f32 v184, v172\n\t" \
    "v_exp_f32 v185, v173\n\t" \
    "v_cvt_pk_bf16_f32 v181, v186, v187\n\t" \
    "v_exp_f32 v186, v174\n\t" \
    "v_exp_f32 v187, v175\n\t" \
    "v_mfma_f32_32x32x16_bf16 v[160:175], v[224:227], v[88:91], 0\n\t" \
    "v_cvt_pk_bf16_f32 v182, v184, v185\n\t" \
    "v_exp_f32 v184, v144\n\t" \
    "v_exp_f32 v185, v145\n\t" \
    "v_cvt_pk_bf16_f32 v183, v186, v187\n\t" \
    "v_exp_f32 v186, v146\n\t" \
    "v_exp_f32 v187, v147\n\t" \
    "v_mfma_f32_32x32x16_bf16 v[16:31], v[236:239], v[180:183], v[16:31]\n\t" \
    "v_cvt_pk_bf16_f32 v176, v184, v185\n\t" \
    "v_exp_f32 v184, v148\n\t" \
    "v_exp_f32 v185, v149\n\t" \
    "v_cvt_pk_bf16_f32 v177, v186, v187\n\t" \
    "v_exp_f32 v186, v150\n\t" \
    "v_exp_f32 v187, v151\n\t" \
    "v_mfma_f32_32x32x16_bf16 v[160:175], v[228:231], v[92:95], v[160:175]\n\t" \
    "v_cvt_pk_bf16_f32 v178, v184, v185\n\t" \
    "v_exp_f32 v184, v152\n\t" \
    "v_exp_f32 v185, v153\n\t" \
    "v_cvt_pk_bf16_f32 v179, v186, v187\n\t" \
    "v_exp_f32 v186, v154\n\t" \
    "v_exp_f32 v187, v155\n\t" \
    "v_mfma_f32_32x32x16_bf16 v[32:47], v[232:235], v[176:179], v[32:47]\n\t" \
    "v_cvt_pk_bf16_f32 v180, v184, v185\n\t" \
    "v_exp_f32 v184, v156\n\t" \
    "v_exp_f32 v185, v157\n\t" \
    "v_cvt_pk_bf16_f32 v181, v186, v187\n\t" \
    "v_exp_f32 v186, v158\n\t" \
    "v_exp_f32 v187, v159\n\t" \
    "s_waitcnt vmcnt(10)\n\t" \
    "global_load_dwordx4 v[224:227], %[lo], s[48:49] offset:0\n\t" \
    "global_load_dwordx4 v[228:231], %[lo], s[50:51] offset:0\n\t" \
    "v_mfma_f32_32x32x16_bf16 v[144:159], v[240:243], v[64:67], 0\n\t" \
    "v_cvt_pk_bf16_f32 v182, v184, v185\n\t" \
    "v_exp_f32 v184, v160\n\t" \
    "v_exp_f32 v185, v161\n\t" \
    "v_cvt_pk_bf16_f32 v183, v186, v187\n\t" \
    "v_exp_f32 v186, v162\n\t" \
    "v_exp_f32 v187, v163\n\t" \
    "v_mfma_f32_32x32x16_bf16 v[32:47], v[236:239], v[180:183], v[32:47]\n\t" \
    "v_cvt_pk_bf16_f32 v176, v184, v185\n\t" \
    "v_exp_f32 v184, v164\n\t" \
    "v_exp_f32 v185, v165\n\t" \
    "v_cvt_pk_bf16_f32 v177, v186, v187\n\t" \
    "v_exp_f32 v186, v166\n\t" \
    "v_exp_f32 v187, v167\n\t" \
    "v_mfma_f32_32x32x16_bf16 v[144:159], v[244:247], v[68:71], v[144:159]\n\t" \
    "v_cvt_pk_bf16_f32 v178, v184, v185\n\t" \
    "v_exp_f32 v184, v168\n\t" \
    "v_exp_f32 v185, v169\n\t" \
    "v_cvt_pk_bf16_f32 v179, v186, v187\n\t" \
    "v_exp_f32 v186, v170\n\t" \
    "v_exp_f32 v187, v171\n\t" \
    "v_mfma_f32_32x32x16_bf16 v[48:63], v[232:235], v[176:179], v[48:63]\n\t" \
    "v_cvt_pk_bf16_f32 v180, v184, v185\n\t" \
    "v_exp_f32 v184, v172\n\t" \
    "v_exp_f32 v185, v173\n\t" \
    "v_cvt_pk_bf16_f32 v181, v186, v187\n\t" \
    "v_exp_f32 v186, v174\n\t" \
    "v_exp_f32 v187, v175\n\t" \
    "v_mfma_f32_32x32x16_bf16 v[160:175], v[240:243], v[72:75], 0\n\t" \
    "v_cvt_pk_bf16_f32 v182, v184, v185\n\t" \
    "v_exp_f32 v184, v144\n\t" \
    "v_exp_f32 v185, v145\n\t" \
    "v_cvt_pk_bf16_f32 v183, v186, v187\n\t" \
    "v_exp_f32 v186, v146\n\t" \
    "v_exp_f32 v187, v147\n\t" \
    "v_mfma_f32_32x32x16_bf16 v[48:63], v[236:239], v[180:183], v[48:63]\n\t" \
    "v_cvt_pk_bf16_f32 v176, v184, v185\n\t" \
    "v_exp_f32 v184, v148\n\t" \
    "v_exp_f32 v185, v149\n\t" \
    "v_cvt_pk_bf16_f32 v177, v186, v187\n\t" \
    "v_exp_f32 v186, v150\n\t" \
    "v_exp_f32 v187, v151\n\t" \
    "v_mfma_f32_32x32x16_bf16 v[160:175], v[244:247], v[76:79], v[160:175]\n\t" \
    "v_cvt_pk_bf16_f32 v178, v184, v185\n\t" \
    "v_exp_f32 v184, v152\n\t" \
    "v_exp_f32 v185, v153\n\t" \
    "v_cvt_pk_bf16_f32 v179, v186, v187\n\t" \
    "v_exp_f32 v186, v154\n\t" \
    "v_exp_f32 v187, v155\n\t" \
    "s_waitcnt vmcnt(10)\n\t" \
    "v_mfma_f32_32x32x16_bf16 v[0:15], v[248:251], v[176:179], v[0:15]\n\t" \
    "v_cvt_pk_bf16_f32 v180, v184, v185\n\t" \
    "v_exp_f32 v184, v156\n\t" \
    "v_exp_f32 v185, v157\n\t" \
    "v_cvt_pk_bf16_f32 v181, v186, v187\n\t" \
    "v_exp_f32 v186, v158\n\t" \
    "v_exp_f32 v187, v159\n\t" \
    "global_load_dwordx4 v[232:235], %[lo], s[48:49] offset:1024\n\t" \
    "global_load_dwordx4 v[236:239], %[lo], s[50:51] offset:1024\n\t" \
    "v_mfma_f32_32x32x16_bf16 v[144:159], v[240:243], v[80:83], 0\n\t" \
    "v_cvt_pk_bf16_f32 v182, v184, v185\n\t" \
    "v_exp_f32 v184, v160\n\t" \
    "v_exp_f32 v185, v161\n\t" \
    "v_cvt_pk_bf16_f32 v183, v186, v187\n\t" \
    "v_exp_f32 v186, v162\n\t" \
    "v_exp_f32 v187, v163\n\t" \
    "v_mfma_f32_32x32x16_bf16 v[0:15], v[252:255], v[180:183], v[0:15]\n\t" \
    "v_cvt_pk_bf16_f32 v176, v184, v185\n\t" \
    "v_exp_f32 v184, v164\n\t" \
    "v_exp_f32 v185, v165\n\t" \
    "v_cvt_pk_bf16_f32 v177, v186, v187\n\t" \
    "v_exp_f32 v186, v166\n\t" \
    "v_exp_f32 v187, v167\n\t" \
    "v_mfma_f32_32x32x16_bf16 v[144:159], v[244:247], v[84:87], v[144:159]\n\t" \
    "v_cvt_pk_bf16_f32 v178, v184, v185\n\t" \
    "v_exp_f32 v184, v168\n\t" \
    "v_exp_f32 v185, v169\n\t" \
    "v_cvt_pk_bf16_f32 v179, v186, v187\n\t" \
    "v_exp_f32 v186, v170\n\t" \
    "v_exp_f32 v187, v171\n\t" \
    "v_mfma_f32_32x32x16_bf16 v[16:31], v[248:251], v[176:179], v[16:31]\n\t" \
    "ds_write_b128 %[la], v[0:3] offset:0\n\t" \
    "v_cvt_pk_bf16_f32 v180, v184, v185\n\t" \
    "v_exp_f32 v184, v172\n\t" \
    "v_exp_f32 v185, v173\n\t" \
    "ds_write_b128 %[la], v[4:7] offset:1024\n\t" \
    "v_cvt_pk_bf16_f32 v181, v186, v187\n\t" \
    "v_exp_f32 v186, v174\n\t" \
    "v_exp_f32 v187, v175\n\t" \
    "v_mfma_f32_32x32x16_bf16 v[160:175], v[240:243], v[88:91], 0\n\t" \
    "ds_write_b128 %[la], v[8:11] offset:2048\n\t" \
    "v_cvt_pk_bf16_f32 v182, v184, v185\n\t" \
    "v_exp_f32 v184, v144\n\t" \
    "v_exp_f32 v185, v145\n\t" \
    "ds_write_b128 %[la], v[12:15] offset:3072\n\t" \
    "v_cvt_pk_bf16_f32 v183, v186, v187\n\t" \
    "v_exp_f32 v186, v146\n\t" \
    "v_exp_f32 v187, v147\n\t" \
    "v_mfma_f32_32x32x16_bf16 v[16:31], v[252:255], v[180:183], v[16:31]\n\t" \
    "v_cvt_pk_bf16_f32 v176, v184, v185\n\t" \
    "v_exp_f32 v184, v148\n\t" \
    "v_exp_f32 v185, v149\n\t" \
    "v_cvt_pk_bf16_f32 v177, v186, v187\n\t" \
    "v_exp_f32 v186, v150\n\t" \
    "v_exp_f32 v187, v151\n\t" \
    "v_mfma_f32_32x32x16_bf16 v[160:175], v[244:247], v[92:95], v[160:175]\n\t" \
    "v_cvt_pk_bf16_f32 v178, v184, v185\n\t" \
    "v_exp_f32 v184, v152\n\t" \
    "v_exp_f32 v185, v153\n\t" \
    "v_cvt_pk_bf16_f32 v179, v186, v187\n\t" \
    "v_exp_f32 v186, v154\n\t" \
    "v_exp_f32 v187, v155\n\t" \
    "v_mfma_f32_32x32x16_bf16 v[32:47], v[248:251], v[176:179], v[32:47]\n\t" \
    "ds_write_b128 %[la], v[16:19] offset:4096\n\t" \
    "v_cvt_pk_bf16_f32 v180, v184, v185\n\t" \
    "v_exp_f32 v184, v156\n\t" \
    "v_exp_f32 v185, v157\n\t" \
    "ds_write_b128 %[la], v[20:23] offset:5120\n\t" \
    "v_cvt_pk_bf16_f32 v181, v186, v187\n\t" \
    "v_exp_f32 v186, v158\n\t" \
    "v_exp_f32 v187, v159\n\t" \
    "global_load_dwordx4 v[240:243], %[lo], s[48:49] offset:2048\n\t" \
    "global_load_dwordx4 v[244:247], %[lo], s[50:51] offset:2048\n\t" \
    "ds_write_b128 %[la], v[24:27] offset:6144\n\t" \
    "v_cvt_pk_bf16_f32 v182, v184, v185\n\t" \
    "v_exp_f32 v184, v160\n\t" \
    "v_exp_f32 v185, v161\n\t" \
    "ds_write_b128 %[la], v[28:31] offset:7168\n\t" \
    "v_cvt_pk_bf16_f32 v183, v186, v187\n\t" \
    "v_exp_f32 v186, v162\n\t" \
    "v_exp_f32 v187, v163\n\t" \
    "v_mfma_f32_32x32x16_bf16 v[32:47], v[252:255], v[180:183], v[32:47]\n\t" \
    "v_cvt_pk_bf16_f32 v176, v184, v185\n\t" \
    "v_exp_f32 v184, v164\n\t" \
    "v_exp_f32 v185, v165\n\t" \
    "v_cvt_pk_bf16_f32 v177, v186, v187\n\t" \
    "v_exp_f32 v186, v166\n\t" \
    "v_exp_f32 v187, v167\n\t" \
    "v_cvt_pk_bf16_f32 v178, v184, v185\n\t" \
    "v_exp_f32 v184, v168\n\t" \
    "v_exp_f32 v185, v169\n\t" \
    "v_cvt_pk_bf16_f32 v179, v186, v187\n\t" \
    "v_exp_f32 v186, v170\n\t" \
    "v_exp_f32 v187, v171\n\t" \
    "v_mfma_f32_32x32x16_bf16 v[48:63], v[248:251], v[176:179], v[48:63]\n\t" \
    "ds_write_b128 %[la], v[32:35] offset:8192\n\t" \
    "v_cvt_pk_bf16_f32 v180, v184, v185\n\t" \
    "v_exp_f32 v184, v172\n\t" \
    "v_exp_f32 v185, v173\n\t" \
    "ds_write_b128 %[la], v[36:39] offset:9216\n\t" \
    "v_cvt_pk_bf16_f32 v181, v186, v187\n\t" \
    "v_exp_f32 v186, v174\n\t" \
    "v_exp_f32 v187, v175\n\t" \
    "s_setprio 3\n\t" \
    "v_cvt_pk_bf16_f32 v182, v184, v185\n\t" \
    "v_cvt_pk_bf16_f32 v183, v186, v187\n\t" \
    "ds_write_b128 %[la], v[40:43] offset:10240\n\t" \
    "ds_write_b128 %[la], v[44:47] offset:11264\n\t" \
    "v_mfma_f32_32x32x16_bf16 v[48:63], v[252:255], v[180:183], v[48:63]\n\t" \
    "global_load_dwordx4 v[248:251], %[lo], s[48:49] offset:3072\n\t" \
    "global_load_dwordx4 v[252:255], %[lo], s[50:51] offset:3072\n\t" \
    "s_nop 9\n\t"
#define ATT_CLOB "v100", "v101", "v102", "v103", "v104", "v105", "v106", "v107", "v108", "v109", "v110", "v111", "v112", "v113", "v114", "v115", "v116", "v117", "v118", "v119", "v120", "v121", "v122", "v123", "v124", "v125", "v126", "v127", "v128", "v129", "v130", "v131", "v144", "v145", "v146", "v147", "v148", "v149", "v150", "v151", "v152", "v153", "v154", "v155", "v156", "v157", "v158", "v159", "v160", "v161", "v162", "v163", "v164", "v165", "v166", "v167", "v168", "v169", "v170", "v171", "v172", "v173", "v174", "v175", "v176", "v177", "v178", "v179", "v180", "v181", "v182", "v183", "v184", "v185", "v186", "v187"
#define GEMM_ASM \
    "s_barrier\n\t" \
    "global_load_dwordx4 v[226:229], v248, %[ws]\n\t" \
    "global_load_dwordx4 v[230:233], v249, %[ws]\n\t" \
    "global_load_dwordx4 v[234:237], v250, %[ws]\n\t" \
    "global_load_dwordx2 v[238:239], v251, %[ws]\n\t" \
    "global_load_dwordx2 v[240:241], v252, %[ws]\n\t" \
    "s_add_u32 m0, %[ring], 0\n\t" \
    "s_nop 0\n\t" \
    "global_load_lds_dwordx4 v200, s[40:41]\n\t" \
    "s_add_u32 m0, %[ring], 1024\n\t" \
    "s_nop 0\n\t" \
    "global_load_lds_dwordx4 v201, s[40:41]\n\t" \
    "s_add_u32 m0, %[ring], 2048\n\t" \
    "s_nop 0\n\t" \
    "global_load_lds_dwordx4 v202, s[40:41]\n\t" \
    "s_add_u32 m0, %[ring], 3072\n\t" \
    "s_nop 0\n\t" \
    "global_load_lds_dwordx4 v203, s[40:41]\n\t" \
    "s_add_u32 m0, %[ring], 4096\n\t" \
    "s_nop 0\n\t" \
    "global_load_lds_dwordx4 v204, s[40:41]\n\t" \
    "s_add_u32 m0, %[ring], 5120\n\t" \
    "s_nop 0\n\t" \
    "global_load_lds_dwordx4 v205, s[40:41]\n\t" \
    "s_add_u32 m0, %[ring], 6144\n\t" \
    "s_nop 0\n\t" \
    "global_load_lds_dwordx4 v206, s[40:41]\n\t" \
    "s_add_u32 m0, %[ring], 7168\n\t" \
    "s_nop 0\n\t" \
    "global_load_lds_dwordx4 v207, s[40:41]\n\t" \
    "s_add_u32 s40, s40, 128\n\t" \
    "s_addc_u32 s41, s41, 0\n\t" \
    "global_load_dwordx4 v[112:115], v208, s[42:43] offset:-3072\n\t" \
    "s_add_u32 m0, %[ring], 8192\n\t" \
    "s_nop 0\n\t" \
    "global_load_lds_dwordx4 v200, s[40:41]\n\t" \
    "global_load_dwordx4 v[116:119], v208, s[42:43] offset:-2048\n\t" \
    "s_add_u32 m0, %[ring], 9216\n\t" \
    "s_nop 0\n\t" \
    "global_load_lds_dwordx4 v201, s[40:41]\n\t" \
    "global_load_dwordx4 v[120:123], v208, s[42:43] offset:-1024\n\t" \
    "s_add_u32 m0, %[ring], 10240\n\t" \
    "s_nop 0\n\t" \
    "global_load_lds_dwordx4 v202, s[40:41]\n\t" \
    "s_waitcnt vmcnt(14)\n\t" \
    "v_mul_f32 v242, 0xc0135761, v240\n\t" \
    "v_mul_f32 v243, 0xbdd2d3e7, v240\n\t" \
    "v_mul_f32 v243, v243, v240\n\t" \
    "s_nop 0\n\t" \
    "v_mul_f32 v243, v243, v240\n\t" \
    "s_nop 0\n\t" \
    "v_lshlrev_b32 v184, 16, v226\n\t" \
    "v_and_b32 v185, 0xffff0000, v226\n\t" \
    "v_lshlrev_b32 v186, 16, v227\n\t" \
    "v_and_b32 v187, 0xffff0000, v227\n\t" \
    "v_lshlrev_b32 v188, 16, v228\n\t" \
    "v_and_b32 v189, 0xffff0000, v228\n\t" \
    "v_lshlrev_b32 v190, 16, v229\n\t" \
    "v_and_b32 v191, 0xffff0000, v229\n\t" \
    "v_mul_f32 v192, v184, v184\n\t" \
    "v_mul_f32 v193, v185, v185\n\t" \
    "global_load_dwordx4 v[124:127], v208, s[42:43] offset:0\n\t" \
    "v_mul_f32 v194, v186, v186\n\t" \
    "v_mul_f32 v195, v187, v187\n\t" \
    "v_mul_f32 v196, v188, v188\n\t" \
    "v_mul_f32 v197, v189, v189\n\t" \
    "v_mul_f32 v198, v190, v190\n\t" \
    "v_mul_f32 v199, v191, v191\n\t" \
    "v_fma_f32 v192, v192, v243, v242\n\t" \
    "v_fma_f32 v193, v193, v243, v242\n\t" \
    "v_fma_f32 v194, v194, v243, v242\n\t" \
    "v_fma_f32 v195, v195, v243, v242\n\t" \
    "v_fma_f32 v196, v196, v243, v242\n\t" \
    "v_fma_f32 v197, v197, v243, v242\n\t" \
    "v_fma_f32 v198, v198, v243, v242\n\t" \
    "v_fma_f32 v199, v199, v243, v242\n\t" \
    "v_mul_f32 v192, v192, v184\n\t" \
    "v_mul_f32 v193, v193, v185\n\t" \
    "s_add_u32 m0, %[ring], 11264\n\t" \
    "s_nop 0\n\t" \
    "global_load_lds_dwordx4 v203, s[40:41]\n\t" \
    "v_mul_f32 v194, v194, v186\n\t" \
    "v_mul_f32 v195, v195, v187\n\t" \
    "v_mul_f32 v196, v196, v188\n\t" \
    "v_mul_f32 v197, v197, v189\n\t" \
    "v_mul_f32 v198, v198, v190\n\t" \
    "v_mul_f32 v199, v199, v191\n\t" \
    "v_exp_f32 v192, v192\n\t" \
    "v_exp_f32 v193, v193\n\t" \
    "v_exp_f32 v194, v194\n\t" \
    "v_exp_f32 v195, v195\n\t" \
    "v_exp_f32 v196, v196\n\t" \
    "v_exp_f32 v197, v197\n\t" \
    "v_exp_f32 v198, v198\n\t" \
    "v_exp_f32 v199, v199\n\t" \
    "v_add_f32 v192, 1.0, v192\n\t" \
    "v_add_f32 v193, 1.0, v193\n\t" \
    "global_load_dwordx4 v[128:131], v208, s[42:43] offset:1024\n\t" \
    "v_add_f32 v194, 1.0, v194\n\t" \
    "v_add_f32 v195, 1.0, v195\n\t" \
    "v_add_f32 v196, 1.0, v196\n\t" \
    "v_add_f32 v197, 1.0, v197\n\t" \
    "v_add_f32 v198, 1.0, v198\n\t" \
    "v_add_f32 v199, 1.0, v199\n\t" \
    "v_rcp_f32 v192, v192\n\t" \
    "v_rcp_f32 v193, v193\n\t" \
    "v_rcp_f32 v194, v194\n\t" \
    "v_rcp_f32 v195, v195\n\t" \
    "v_rcp_f32 v196, v196\n\t" \
    "v_rcp_f32 v197, v197\n\t" \
    "v_rcp_f32 v198, v198\n\t" \
    "v_rcp_f32 v199, v199\n\t" \
    "v_mul_f32 v184, v184, v240\n\t" \
    "v_mul_f32 v185, v185, v240\n\t" \
    "global_load_dwordx4 v[132:135], v208, s[42:43] offset:2048\n\t" \
    "v_mul_f32 v186, v186, v240\n\t" \
    "v_mul_f32 v187, v187, v240\n\t" \
    "v_mul_f32 v188, v188, v240\n\t" \
    "v_mul_f32 v189, v189, v240\n\t" \
    "v_mul_f32 v190, v190, v240\n\t" \
    "v_mul_f32 v191, v191, v240\n\t" \
    "v_mul_f32 v212, v192, v184\n\t" \
    "v_mul_f32 v213, v193, v185\n\t" \
    "v_mul_f32 v214, v194, v186\n\t" \
    "v_mul_f32 v215, v195, v187\n\t" \
    "v_mul_f32 v216, v196, v188\n\t" \
    "v_mul_f32 v217, v197, v189\n\t" \
    "v_mul_f32 v218, v198, v190\n\t" \
    "v_mul_f32 v219, v199, v191\n\t" \
    "v_lshlrev_b32 v184, 16, v230\n\t" \
    "v_and_b32 v185, 0xffff0000, v230\n\t" \
    "global_load_dwordx4 v[136:139], v208, s[42:43] offset:3072\n\t" \
    "s_add_u32 s42, s42, 7168\n\t" \
    "s_addc_u32 s43, s43, 0\n\t" \
    "v_lshlrev_b32 v186, 16, v231\n\t" \
    "v_and_b32 v187, 0xffff0000, v231\n\t" \
    "v_lshlrev_b32 v188, 16, v232\n\t" \
    "v_and_b32 v189, 0xffff0000, v232\n\t" \
    "v_lshlrev_b32 v190, 16, v233\n\t" \
    "v_and_b32 v191, 0xffff0000, v233\n\t" \
    "v_mul_f32 v192, v184, v184\n\t" \
    "v_mul_f32 v193, v185, v185\n\t" \
    "v_mul_f32 v194, v186, v186\n\t" \
    "v_mul_f32 v195, v187, v187\n\t" \
    "v_mul_f32 v196, v188, v188\n\t" \
    "v_mul_f32 v197, v189, v189\n\t" \
    "v_mul_f32 v198, v190, v190\n\t" \
    "v_mul_f32 v199, v191, v191\n\t" \
    "v_fma_f32 v192, v192, v243, v242\n\t" \
    "v_fma_f32 v193, v193, v243, v242\n\t" \
    "global_load_dwordx4 v[140:143], v208, s[42:43] offset:-3072\n\t" \
    "v_fma_f32 v194, v194, v243, v242\n\t" \
    "v_fma_f32 v195, v195, v243, v242\n\t" \
    "v_fma_f32 v196, v196, v243, v242\n\t" \
    "v_fma_f32 v197, v197, v243, v242\n\t" \
    "v_fma_f32 v198, v198, v243, v242\n\t" \
    "v_fma_f32 v199, v199, v243, v242\n\t" \
    "v_mul_f32 v192, v192, v184\n\t" \
    "v_mul_f32 v193, v193, v185\n\t" \
    "v_mul_f32 v194, v194, v186\n\t" \
    "v_mul_f32 v195, v195, v187\n\t" \
    "v_mul_f32 v196, v196, v188\n\t" \
    "v_mul_f32 v197, v197, v189\n\t" \
    "v_mul_f32 v198, v198, v190\n\t" \
    "v_mul_f32 v199, v199, v191\n\t" \
    "v_exp_f32 v192, v192\n\t" \
    "v_exp_f32 v193, v193\n\t" \
    "s_add_u32 m0, %[ring], 12288\n\t" \
    "s_nop 0\n\t" \
    "global_load_lds_dwordx4 v204, s[40:41]\n\t" \
    "v_exp_f32 v194, v194\n\t" \
    "v_exp_f32 v195, v195\n\t" \
    "v_exp_f32 v196, v196\n\t" \
    "v_exp_f32 v197, v197\n\t" \
    "v_exp_f32 v198, v198\n\t" \
    "v_exp_f32 v199, v199\n\t" \
    "v_add_f32 v192, 1.0, v192\n\t" \
    "v_add_f32 v193, 1.0, v193\n\t" \
    "v_add_f32 v194, 1.0, v194\n\t" \
    "v_add_f32 v195, 1.0, v195\n\t" \
    "v_add_f32 v196, 1.0, v196\n\t" \
    "v_add_f32 v197, 1.0, v197\n\t" \
    "v_add_f32 v198, 1.0, v198\n\t" \
    "v_add_f32 v199, 1.0, v199\n\t" \
    "v_rcp_f32 v192, v192\n\t" \
    "v_rcp_f32 v193, v193\n\t" \
    "global_load_dwordx4 v[144:147], v208, s[42:43] offset:-2048\n\t" \
    "v_rcp_f32 v194, v194\n\t" \
    "v_rcp_f32 v195, v195\n\t" \
    "v_rcp_f32 v196, v196\n\t" \
    "v_rcp_f32 v197, v197\n\t" \
    "v_rcp_f32 v198, v198\n\t" \
    "v_rcp_f32 v199, v199\n\t" \
    "v_mul_f32 v184, v184, v240\n\t" \
    "v_mul_f32 v185, v185, v240\n\t" \
    "v_mul_f32 v186, v186, v240\n\t" \
    "v_mul_f32 v187, v187, v240\n\t" \
    "v_mul_f32 v188, v188, v240\n\t" \
    "v_mul_f32 v189, v189, v240\n\t" \
    "v_mul_f32 v190, v190, v240\n\t" \
    "v_mul_f32 v191, v191, v240\n\t" \
    "v_mul_f32 v220, v192, v184\n\t" \
    "v_mul_f32 v221, v193, v185\n\t" \
    "s_add_u32 m0, %[ring], 13312\n\t" \
    "s_nop 0\n\t" \
    "global_load_lds_dwordx4 v205, s[40:41]\n\t" \
    "v_mul_f32 v222, v194, v186\n\t" \
    "v_mul_f32 v223, v195, v187\n\t" \
    "v_mul_f32 v224, v196, v188\n\t" \
    "v_mul_f32 v225, v197, v189\n\t" \
    "v_mul_f32 v226, v198, v190\n\t" \
    "v_mul_f32 v227, v199, v191\n\t" \
    "v_lshlrev_b32 v184, 16, v234\n\t" \
    "v_and_b32 v185, 0xffff0000, v234\n\t" \
    "v_lshlrev_b32 v186, 16, v235\n\t" \
    "v_and_b32 v187, 0xffff0000, v235\n\t" \
    "v_lshlrev_b32 v188, 16, v236\n\t" \
    "v_and_b32 v189, 0xffff0000, v236\n\t" \
    "v_lshlrev_b32 v190, 16, v237\n\t" \
    "v_and_b32 v191, 0xffff0000, v237\n\t" \
    "v_mul_f32 v192, v184, v184\n\t" \
    "v_mul_f32 v193, v185, v185\n\t" \
    "global_load_dwordx4 v[148:151], v208, s[42:43] offset:-1024\n\t" \
    "v_mul_f32 v194, v186, v186\n\t" \
    "v_mul_f32 v195, v187, v187\n\t" \
    "v_mul_f32 v196, v188, v188\n\t" \
    "v_mul_f32 v197, v189, v189\n\t" \
    "v_mul_f32 v198, v190, v190\n\t" \
    "v_mul_f32 v199, v191, v191\n\t" \
    "v_fma_f32 v192, v192, v243, v242\n\t" \
    "v_fma_f32 v193, v193, v243, v242\n\t" \
    "v_fma_f32 v194, v194, v243, v242\n\t" \
    "v_fma_f32 v195, v195, v243, v242\n\t" \
    "v_fma_f32 v196, v196, v243, v242\n\t" \
    "v_fma_f32 v197, v197, v243, v242\n\t" \
    "v_fma_f32 v198, v198, v243, v242\n\t" \
    "v_fma_f32 v199, v199, v243, v242\n\t" \
    "v_mul_f32 v192, v192, v184\n\t" \
    "v_mul_f32 v193, v193, v185\n\t" \
    "s_add_u32 m0, %[ring], 14336\n\t" \
    "s_nop 0\n\t" \
    "global_load_lds_dwordx4 v206, s[40:41]\n\t" \
    "v_mul_f32 v194, v194, v186\n\t" \
    "v_mul_f32 v195, v195, v187\n\t" \
    "v_mul_f32 v196, v196, v188\n\t" \
    "v_mul_f32 v197, v197, v189\n\t" \
    "v_mul_f32 v198, v198, v190\n\t" \
    "v_mul_f32 v199, v199, v191\n\t" \
    "v_exp_f32 v192, v192\n\t" \
    "v_exp_f32 v193, v193\n\t" \
    "v_exp_f32 v194, v194\n\t" \
    "v_exp_f32 v195, v195\n\t" \
    "v_exp_f32 v196, v196\n\t" \
    "v_exp_f32 v197, v197\n\t" \
    "v_exp_f32 v198, v198\n\t" \
    "v_exp_f32 v199, v199\n\t" \
    "v_add_f32 v192, 1.0, v192\n\t" \
    "v_add_f32 v193, 1.0, v193\n\t" \
    "global_load_dwordx4 v[152:155], v208, s[42:43] offset:0\n\t" \
    "v_add_f32 v194, 1.0, v194\n\t" \
    "v_add_f32 v195, 1.0, v195\n\t" \
    "v_add_f32 v196, 1.0, v196\n\t" \
    "v_add_f32 v197, 1.0, v197\n\t" \
    "v_add_f32 v198, 1.0, v198\n\t" \
    "v_add_f32 v199, 1.0, v199\n\t" \
    "v_rcp_f32 v192, v192\n\t" \
    "v_rcp_f32 v193, v193\n\t" \
    "v_rcp_f32 v194, v194\n\t" \
    "v_rcp_f32 v195, v195\n\t" \
    "v_rcp_f32 v196, v196\n\t" \
    "v_rcp_f32 v197, v197\n\t" \
    "v_rcp_f32 v198, v198\n\t" \
    "v_rcp_f32 v199, v199\n\t" \
    "v_mul_f32 v184, v184, v240\n\t" \
    "v_mul_f32 v185, v185, v240\n\t" \
    "s_add_u32 m0, %[ring], 15360\n\t" \
    "s_nop 0\n\t" \
    "global_load_lds_dwordx4 v207, s[40:41]\n\t" \
    "v_mul_f32 v186, v186, v240\n\t" \
    "v_mul_f32 v187, v187, v240\n\t" \
    "v_mul_f32 v188, v188, v240\n\t" \
    "v_mul_f32 v189, v189, v240\n\t" \
    "v_mul_f32 v190, v190, v240\n\t" \
    "v_mul_f32 v191, v191, v240\n\t" \
    "v_mul_f32 v228, v192, v184\n\t" \
    "v_mul_f32 v229, v193, v185\n\t" \
    "v_mul_f32 v230, v194, v186\n\t" \
    "v_mul_f32 v231, v195, v187\n\t" \
    "v_mul_f32 v232, v196, v188\n\t" \
    "v_mul_f32 v233, v197, v189\n\t" \
    "v_mul_f32 v234, v198, v190\n\t" \
    "v_mul_f32 v235, v199, v191\n\t" \
    "v_lshlrev_b32 v184, 16, v238\n\t" \
    "v_and_b32 v185, 0xffff0000, v238\n\t" \
    "global_load_dwordx4 v[156:159], v208, s[42:43] offset:1024\n\t" \
    "v_lshlrev_b32 v186, 16, v239\n\t" \
    "v_and_b32 v187, 0xffff0000, v239\n\t" \
    "v_mul_f32 v192, v184, v184\n\t" \
    "v_mul_f32 v193, v185, v185\n\t" \
    "v_mul_f32 v194, v186, v186\n\t" \
    "v_mul_f32 v195, v187, v187\n\t" \
    "v_fma_f32 v192, v192, v243, v242\n\t" \
    "v_fma_f32 v193, v193, v243, v242\n\t" \
    "v_fma_f32 v194, v194, v243, v242\n\t" \
    "v_fma_f32 v195, v195, v243, v242\n\t" \
    "v_mul_f32 v192, v192, v184\n\t" \
    "v_mul_f32 v193, v193, v185\n\t" \
    "v_mul_f32 v194, v194, v186\n\t" \
    "v_mul_f32 v195, v195, v187\n\t" \
    "v_exp_f32 v192, v192\n\t" \
    "v_exp_f32 v193, v193\n\t" \
    "global_load_dwordx4 v[160:163], v208, s[42:43] offset:2048\n\t" \
    "v_exp_f32 v194, v194\n\t" \
    "v_exp_f32 v195, v195\n\t" \
    "v_add_f32 v192, 1.0, v192\n\t" \
    "v_add_f32 v193, 1.0, v193\n\t" \
    "v_add_f32 v194, 1.0, v194\n\t" \
    "v_add_f32 v195, 1.0, v195\n\t" \
    "v_rcp_f32 v192, v192\n\t" \
    "v_rcp_f32 v193, v193\n\t" \
    "v_rcp_f32 v194, v194\n\t" \
    "v_rcp_f32 v195, v195\n\t" \
    "v_mul_f32 v184, v184, v240\n\t" \
    "v_mul_f32 v185, v185, v240\n\t" \
    "v_mul_f32 v186, v186, v240\n\t" \
    "v_mul_f32 v187, v187, v240\n\t" \
    "v_mul_f32 v236, v192, v184\n\t" \
    "v_mul_f32 v237, v193, v185\n\t" \
    "global_load_dwordx4 v[164:167], v208, s[42:43] offset:3072\n\t" \
    "s_add_u32 s42, s42, 7168\n\t" \
    "s_addc_u32 s43, s43, 0\n\t" \
    "v_mul_f32 v238, v194, v186\n\t" \
    "v_mul_f32 v239, v195, v187\n\t" \
    "s_add_u32 s40, s40, 128\n\t" \
    "s_addc_u32 s41, s41, 0\n\t" \
    "s_waitcnt vmcnt(22)\n\t" \
    "ds_read_b128 v[168:171], v210 offset:0\n\t" \
    "ds_read_b128 v[172:175], v210 offset:2048\n\t" \
    "ds_read_b128 v[176:179], v210 offset:4096\n\t" \
    "ds_read_b128 v[180:183], v210 offset:6144\n\t" \
    "s_waitcnt vmcnt(17)\n\t" \
    "s_waitcnt lgkmcnt(0)\n\t" \
    "v_mfma_f32_16x16x32_bf16 v[0:3], v[112:115], v[168:171], 0\n\t" \
    "v_mfma_f32_16x16x32_bf16 v[28:31], v[112:115], v[172:175], 0\n\t" \
    "ds_read_b128 v[184:187], v211 offset:0\n\t" \
    "ds_read_b128 v[188:191], v211 offset:2048\n\t" \
    "ds_read_b128 v[192:195], v211 offset:4096\n\t" \
    "ds_read_b128 v[196:199], v211 offset:6144\n\t" \
    "v_mfma_f32_16x16x32_bf16 v[56:59], v[112:115], v[176:179], 0\n\t" \
    "v_mfma_f32_16x16x32_bf16 v[84:87], v[112:115], v[180:183], 0\n\t" \
    "global_load_dwordx4 v[112:115], v208, s[42:43] offset:-3072\n\t" \
    "v_mfma_f32_16x16x32_bf16 v[4:7], v[116:119], v[168:171], 0\n\t" \
    "v_mfma_f32_16x16x32_bf16 v[32:35], v[116:119], v[172:175], 0\n\t" \
    "s_add_u32 m0, %[ring], 16384\n\t" \
    "s_nop 0\n\t" \
    "global_load_lds_dwordx4 v200, s[40:41]\n\t" \
    "v_mfma_f32_16x16x32_bf16 v[60:63], v[116:119], v[176:179], 0\n\t" \
    "v_mfma_f32_16x16x32_bf16 v[88:91], v[116:119], v[180:183], 0\n\t" \
    "global_load_dwordx4 v[116:119], v208, s[42:43] offset:-2048\n\t" \
    "v_mfma_f32_16x16x32_bf16 v[8:11], v[120:123], v[168:171], 0\n\t" \
    "v_mfma_f32_16x16x32_bf16 v[36:39], v[120:123], v[172:175], 0\n\t" \
    "s_add_u32 m0, %[ring], 17408\n\t" \
    "s_nop 0\n\t" \
    "global_load_lds_dwordx4 v201, s[40:41]\n\t" \
    "v_mfma_f32_16x16x32_bf16 v[64:67], v[120:123], v[176:179], 0\n\t" \
    "v_mfma_f32_16x16x32_bf16 v[92:95], v[120:123], v[180:183], 0\n\t" \
    "global_load_dwordx4 v[120:123], v208, s[42:43] offset:-1024\n\t" \
    "s_waitcnt vmcnt(16)\n\t" \
    "v_mfma_f32_16x16x32_bf16 v[12:15], v[124:127], v[168:171], 0\n\t" \
    "v_mfma_f32_16x16x32_bf16 v[40:43], v[124:127], v[172:175], 0\n\t" \
    "s_add_u32 m0, %[ring], 18432\n\t" \
    "s_nop 0\n\t" \
    "global_load_lds_dwordx4 v202, s[40:41]\n\t" \
    "v_mfma_f32_16x16x32_bf16 v[68:71], v[124:127], v[176:179], 0\n\t" \
    "v_mfma_f32_16x16x32_bf16 v[96:99], v[124:127], v[180:183], 0\n\t" \
    "global_load_dwordx4 v[124:127], v208, s[42:43] offset:0\n\t" \
    "v_mfma_f32_16x16x32_bf16 v[16:19], v[128:131], v[168:171], 0\n\t" \
    "v_mfma_f32_16x16x32_bf16 v[44:47], v[128:131], v[172:175], 0\n\t" \
    "s_add_u32 m0, %[ring], 19456\n\t" \
    "s_nop 0\n\t" \
    "global_load_lds_dwordx4 v203, s[40:41]\n\t" \
    "v_mfma_f32_16x16x32_bf16 v[72:75], v[128:131], v[176:179], 0\n\t" \
    "v_mfma_f32_16x16x32_bf16 v[100:103], v[128:131], v[180:183], 0\n\t" \
    "global_load_dwordx4 v[128:131], v208, s[42:43] offset:1024\n\t" \
    "v_mfma_f32_16x16x32_bf16 v[20:23], v[132:135], v[168:171], 0\n\t" \
    "v_mfma_f32_16x16x32_bf16 v[48:51], v[132:135], v[172:175], 0\n\t" \
    "v_mfma_f32_16x16x32_bf16 v[76:79], v[132:135], v[176:179], 0\n\t" \
    "v_mfma_f32_16x16x32_bf16 v[104:107], v[132:135], v[180:183], 0\n\t" \
    "global_load_dwordx4 v[132:135], v208, s[42:43] offset:2048\n\t" \
    "v_mfma_f32_16x16x32_bf16 v[24:27], v[136:139], v[168:171], 0\n\t" \
    "v_mfma_f32_16x16x32_bf16 v[52:55], v[136:139], v[172:175], 0\n\t" \
    "v_mfma_f32_16x16x32_bf16 v[80:83], v[136:139], v[176:179], 0\n\t" \
    "v_mfma_f32_16x16x32_bf16 v[108:111], v[136:139], v[180:183], 0\n\t" \
    "global_load_dwordx4 v[136:139], v208, s[42:43] offset:3072\n\t" \
    "s_add_u32 s42, s42, 7168\n\t" \
    "s_addc_u32 s43, s43, 0\n\t" \
    "s_waitcnt vmcnt(14)\n\t" \
    "s_waitcnt lgkmcnt(0)\n\t" \
    "v_mfma_f32_16x16x32_bf16 v[0:3], v[140:143], v[184:187], v[0:3]\n\t" \
    "v_mfma_f32_16x16x32_bf16 v[28:31], v[140:143], v[188:191], v[28:31]\n\t" \
    "ds_read_b128 v[168:171], v210 offset:8192\n\t" \
    "ds_read_b128 v[172:175], v210 offset:10240\n\t" \
    "ds_read_b128 v[176:179], v210 offset:12288\n\t" \
    "ds_read_b128 v[180:183], v210 offset:14336\n\t" \
    "v_mfma_f32_16x16x32_bf16 v[56:59], v[140:143], v[192:195], v[56:59]\n\t" \
    "v_mfma_f32_16x16x32_bf16 v[84:87], v[140:143], v[196:199], v[84:87]\n\t" \
    "global_load_dwordx4 v[140:143], v208, s[42:43] offset:-3072\n\t" \
    "v_mfma_f32_16x16x32_bf16 v[4:7], v[144:147], v[184:187], v[4:7]\n\t" \
    "v_mfma_f32_16x16x32_bf16 v[32:35], v[144:147], v[188:191], v[32:35]\n\t" \
    "s_add_u32 m0, %[ring], 20480\n\t" \
    "s_nop 0\n\t" \
    "global_load_lds_dwordx4 v204, s[40:41]\n\t" \
    "v_mfma_f32_16x16x32_bf16 v[60:63], v[144:147], v[192:195], v[60:63]\n\t" \
    "v_mfma_f32_16x16x32_bf16 v[88:91], v[144:147], v[196:199], v[88:91]\n\t" \
    "global_load_dwordx4 v[144:147], v208, s[42:43] offset:-2048\n\t" \
    "v_mfma_f32_16x16x32_bf16 v[8:11], v[148:151], v[184:187], v[8:11]\n\t" \
    "v_mfma_f32_16x16x32_bf16 v[36:39], v[148:151], v[188:191], v[36:39]\n\t" \
    "s_add_u32 m0, %[ring], 21504\n\t" \
    "s_nop 0\n\t" \
    "global_load_lds_dwordx4 v205, s[40:41]\n\t" \
    "v_mfma_f32_16x16x32_bf16 v[64:67], v[148:151], v[192:195], v[64:67]\n\t" \
    "v_mfma_f32_16x16x32_bf16 v[92:95], v[148:151], v[196:199], v[92:95]\n\t" \
    "global_load_dwordx4 v[148:151], v208, s[42:43] offset:-1024\n\t" \
    "s_waitcnt vmcnt(16)\n\t" \
    "v_mfma_f32_16x16x32_bf16 v[12:15], v[152:155], v[184:187], v[12:15]\n\t" \
    "v_mfma_f32_16x16x32_bf16 v[40:43], v[152:155], v[188:191], v[40:43]\n\t" \
    "s_add_u32 m0, %[ring], 22528\n\t" \
    "s_nop 0\n\t" \
    "global_load_lds_dwordx4 v206, s[40:41]\n\t" \
    "v_mfma_f32_16x16x32_bf16 v[68:71], v[152:155], v[192:195], v[68:71]\n\t" \
    "v_mfma_f32_16x16x32_bf16 v[96:99], v[152:155], v[196:199], v[96:99]\n\t" \
    "global_load_dwordx4 v[152:155], v208, s[42:43] offset:0\n\t" \
    "v_mfma_f32_16x16x32_bf16 v[16:19], v[156:159], v[184:187], v[16:19]\n\t" \
    "v_mfma_f32_16x16x32_bf16 v[44:47], v[156:159], v[188:191], v[44:47]\n\t" \
    "s_add_u32 m0, %[ring], 23552\n\t" \
    "s_nop 0\n\t" \
    "global_load_lds_dwordx4 v207, s[40:41]\n\t" \
    "v_mfma_f32_16x16x32_bf16 v[72:75], v[156:159], v[192:195], v[72:75]\n\t" \
    "v_mfma_f32_16x16x32_bf16 v[100:103], v[156:159], v[196:199], v[100:103]\n\t" \
    "global_load_dwordx4 v[156:159], v208, s[42:43] offset:1024\n\t" \
    "v_mfma_f32_16x16x32_bf16 v[20:23], v[160:163], v[184:187], v[20:23]\n\t" \
    "v_mfma_f32_16x16x32_bf16 v[48:51], v[160:163], v[188:191], v[48:51]\n\t" \
    "v_mfma_f32_16x16x32_bf16 v[76:79], v[160:163], v[192:195], v[76:79]\n\t" \
    "v_mfma_f32_16x16x32_bf16 v[104:107], v[160:163], v[196:199], v[104:107]\n\t" \
    "global_load_dwordx4 v[160:163], v208, s[42:43] offset:2048\n\t" \
    "v_mfma_f32_16x16x32_bf16 v[24:27], v[164:167], v[184:187], v[24:27]\n\t" \
    "v_mfma_f32_16x16x32_bf16 v[52:55], v[164:167], v[188:191], v[52:55]\n\t" \
    "v_mfma_f32_16x16x32_bf16 v[80:83], v[164:167], v[192:195], v[80:83]\n\t" \
    "v_mfma_f32_16x16x32_bf16 v[108:111], v[164:167], v[196:199], v[108:111]\n\t" \
    "global_load_dwordx4 v[164:167], v208, s[42:43] offset:3072\n\t" \
    "s_add_u32 s42, s42, 7168\n\t" \
    "s_addc_u32 s43, s43, 0\n\t" \
    "s_add_u32 s40, s40, 128\n\t" \
    "s_addc_u32 s41, s41, 0\n\t" \
    "s_waitcnt vmcnt(17)\n\t" \
    "s_waitcnt lgkmcnt(0)\n\t" \
    "v_mfma_f32_16x16x32_bf16 v[0:3], v[112:115], v[168:171], v[0:3]\n\t" \
    "v_mfma_f32_16x16x32_bf16 v[28:31], v[112:115], v[172:175], v[28:31]\n\t" \
    "ds_read_b128 v[184:187], v211 offset:8192\n\t" \
    "ds_read_b128 v[188:191], v211 offset:10240\n\t" \
    "ds_read_b128 v[192:195], v211 offset:12288\n\t" \
    "ds_read_b128 v[196:199], v211 offset:14336\n\t" \
    "v_mfma_f32_16x16x32_bf16 v[56:59], v[112:115], v[176:179], v[56:59]\n\t" \
    "v_mfma_f32_16x16x32_bf16 v[84:87], v[112:115], v[180:183], v[84:87]\n\t" \
    "global_load_dwordx4 v[112:115], v208, s[42:43] offset:-3072\n\t" \
    "v_mfma_f32_16x16x32_bf16 v[4:7], v[116:119], v[168:171], v[4:7]\n\t" \
    "v_mfma_f32_16x16x32_bf16 v[32:35], v[116:119], v[172:175], v[32:35]\n\t" \
    "s_add_u32 m0, %[ring], 0\n\t" \
    "s_nop 0\n\t" \
    "global_load_lds_dwordx4 v200, s[40:41]\n\t" \
    "v_mfma_f32_16x16x32_bf16 v[60:63], v[116:119], v[176:179], v[60:63]\n\t" \
    "v_mfma_f32_16x16x32_bf16 v[88:91], v[116:119], v[180:183], v[88:91]\n\t" \
    "global_load_dwordx4 v[116:119], v208, s[42:43] offset:-2048\n\t" \
    "v_mfma_f32_16x16x32_bf16 v[8:11], v[120:123], v[168:171], v[8:11]\n\t" \
    "v_mfma_f32_16x16x32_bf16 v[36:39], v[120:123], v[172:175], v[36:39]\n\t" \
    "s_add_u32 m0, %[ring], 1024\n\t" \
    "s_nop 0\n\t" \
    "global_load_lds_dwordx4 v201, s[40:41]\n\t" \
    "v_mfma_f32_16x16x32_bf16 v[64:67], v[120:123], v[176:179], v[64:67]\n\t" \
    "v_mfma_f32_16x16x32_bf16 v[92:95], v[120:123], v[180:183], v[92:95]\n\t" \
    "global_load_dwordx4 v[120:123], v208, s[42:43] offset:-1024\n\t" \
    "s_waitcnt vmcnt(16)\n\t" \
    "v_mfma_f32_16x16x32_bf16 v[12:15], v[124:127], v[168:171], v[12:15]\n\t" \
    "v_mfma_f32_16x16x32_bf16 v[40:43], v[124:127], v[172:175], v[40:43]\n\t" \
    "s_add_u32 m0, %[ring], 2048\n\t" \
    "s_nop 0\n\t" \
    "global_load_lds_dwordx4 v202, s[40:41]\n\t" \
    "v_mfma_f32_16x16x32_bf16 v[68:71], v[124:127], v[176:179], v[68:71]\n\t" \
    "v_mfma_f32_16x16x32_bf16 v[96:99], v[124:127], v[180:183], v[96:99]\n\t" \
    "global_load_dwordx4 v[124:127], v208, s[42:43] offset:0\n\t" \
    "v_mfma_f32_16x16x32_bf16 v[16:19], v[128:131], v[168:171], v[16:19]\n\t" \
    "v_mfma_f32_16x16x32_bf16 v[44:47], v[128:131], v[172:175], v[44:47]\n\t" \
    "s_add_u32 m0, %[ring], 3072\n\t" \
    "s_nop 0\n\t" \
    "global_load_lds_dwordx4 v203, s[40:41]\n\t" \
    "v_mfma_f32_16x16x32_bf16 v[72:75], v[128:131], v[176:179], v[72:75]\n\t" \
    "v_mfma_f32_16x16x32_bf16 v[100:103], v[128:131], v[180:183], v[100:103]\n\t" \
    "global_load_dwordx4 v[128:131], v208, s[42:43] offset:1024\n\t" \
    "v_mfma_f32_16x16x32_bf16 v[20:23], v[132:135], v[168:171], v[20:23]\n\t" \
    "v_mfma_f32_16x16x32_bf16 v[48:51], v[132:135], v[172:175], v[48:51]\n\t" \
    "v_mfma_f32_16x16x32_bf16 v[76:79], v[132:135], v[176:179], v[76:79]\n\t" \
    "v_mfma_f32_16x16x32_bf16 v[104:107], v[132:135], v[180:183], v[104:107]\n\t" \
    "global_load_dwordx4 v[132:135], v208, s[42:43] offset:2048\n\t" \
    "v_mfma_f32_16x16x32_bf16 v[24:27], v[136:139], v[168:171], v[24:27]\n\t" \
    "v_mfma_f32_16x16x32_bf16 v[52:55], v[136:139], v[172:175], v[52:55]\n\t" \
    "v_mfma_f32_16x16x32_bf16 v[80:83], v[136:139], v[176:179], v[80:83]\n\t" \
    "v_mfma_f32_16x16x32_bf16 v[108:111], v[136:139], v[180:183], v[108:111]\n\t" \
    "global_load_dwordx4 v[136:139], v208, s[42:43] offset:3072\n\t" \
    "s_add_u32 s42, s42, 7168\n\t" \
    "s_addc_u32 s43, s43, 0\n\t" \
    "s_waitcnt vmcnt(14)\n\t" \
    "s_waitcnt lgkmcnt(0)\n\t" \
    "v_mfma_f32_16x16x32_bf16 v[0:3], v[140:143], v[184:187], v[0:3]\n\t" \
    "v_mfma_f32_16x16x32_bf16 v[28:31], v[140:143], v[188:191], v[28:31]\n\t" \
    "ds_read_b128 v[168:171], v210 offset:16384\n\t" \
    "ds_read_b128 v[172:175], v210 offset:18432\n\t" \
    "ds_read_b128 v[176:179], v210 offset:20480\n\t" \
    "ds_read_b128 v[180:183], v210 offset:22528\n\t" \
    "v_mfma_f32_16x16x32_bf16 v[56:59], v[140:143], v[192:195], v[56:59]\n\t" \
    "v_mfma_f32_16x16x32_bf16 v[84:87], v[140:143], v[196:199], v[84:87]\n\t" \
    "global_load_dwordx4 v[140:143], v208, s[42:43] offset:-3072\n\t" \
    "v_mfma_f32_16x16x32_bf16 v[4:7], v[144:147], v[184:187], v[4:7]\n\t" \
    "v_mfma_f32_16x16x32_bf16 v[32:35], v[144:147], v[188:191], v[32:35]\n\t" \
    "s_add_u32 m0, %[ring], 4096\n\t" \
    "s_nop 0\n\t" \
    "global_load_lds_dwordx4 v204, s[40:41]\n\t" \
    "v_mfma_f32_16x16x32_bf16 v[60:63], v[144:147], v[192:195], v[60:63]\n\t" \
    "v_mfma_f32_16x16x32_bf16 v[88:91], v[144:147], v[196:199], v[88:91]\n\t" \
    "global_load_dwordx4 v[144:147], v208, s[42:43] offset:-2048\n\t" \
    "v_mfma_f32_16x16x32_bf16 v[8:11], v[148:151], v[184:187], v[8:11]\n\t" \
    "v_mfma_f32_16x16x32_bf16 v[36:39], v[148:151], v[188:191], v[36:39]\n\t" \
    "s_add_u32 m0, %[ring], 5120\n\t" \
    "s_nop 0\n\t" \
    "global_load_lds_dwordx4 v205, s[40:41]\n\t" \
    "v_mfma_f32_16x16x32_bf16 v[64:67], v[148:151], v[192:195], v[64:67]\n\t" \
    "v_mfma_f32_16x16x32_bf16 v[92:95], v[148:151], v[196:199], v[92:95]\n\t" \
    "global_load_dwordx4 v[148:151], v208, s[42:43] offset:-1024\n\t" \
    "s_waitcnt vmcnt(16)\n\t" \
    "v_mfma_f32_16x16x32_bf16 v[12:15], v[152:155], v[184:187], v[12:15]\n\t" \
    "v_mfma_f32_16x16x32_bf16 v[40:43], v[152:155], v[188:191], v[40:43]\n\t" \
    "s_add_u32 m0, %[ring], 6144\n\t" \
    "s_nop 0\n\t" \
    "global_load_lds_dwordx4 v206, s[40:41]\n\t" \
    "v_mfma_f32_16x16x32_bf16 v[68:71], v[152:155], v[192:195], v[68:71]\n\t" \
    "v_mfma_f32_16x16x32_bf16 v[96:99], v[152:155], v[196:199], v[96:99]\n\t" \
    "global_load_dwordx4 v[152:155], v208, s[42:43] offset:0\n\t" \
    "v_mfma_f32_16x16x32_bf16 v[16:19], v[156:159], v[184:187], v[16:19]\n\t" \
    "v_mfma_f32_16x16x32_bf16 v[44:47], v[156:159], v[188:191], v[44:47]\n\t" \
    "s_add_u32 m0, %[ring], 7168\n\t" \
    "s_nop 0\n\t" \
    "global_load_lds_dwordx4 v207, s[40:41]\n\t" \
    "v_mfma_f32_16x16x32_bf16 v[72:75], v[156:159], v[192:195], v[72:75]\n\t" \
    "v_mfma_f32_16x16x32_bf16 v[100:103], v[156:159], v[196:199], v[100:103]\n\t" \
    "global_load_dwordx4 v[156:159], v208, s[42:43] offset:1024\n\t" \
    "v_mfma_f32_16x16x32_bf16 v[20:23], v[160:163], v[184:187], v[20:23]\n\t" \
    "v_mfma_f32_16x16x32_bf16 v[48:51], v[160:163], v[188:191], v[48:51]\n\t" \
    "v_mfma_f32_16x16x32_bf16 v[76:79], v[160:163], v[192:195], v[76:79]\n\t" \
    "v_mfma_f32_16x16x32_bf16 v[104:107], v[160:163], v[196:199], v[104:107]\n\t" \
    "global_load_dwordx4 v[160:163], v208, s[42:43] offset:2048\n\t" \
    "v_mfma_f32_16x16x32_bf16 v[24:27], v[164:167], v[184:187], v[24:27]\n\t" \
    "v_mfma_f32_16x16x32_bf16 v[52:55], v[164:167], v[188:191], v[52:55]\n\t" \
    "v_mfma_f32_16x16x32_bf16 v[80:83], v[164:167], v[192:195], v[80:83]\n\t" \
    "v_mfma_f32_16x16x32_bf16 v[108:111], v[164:167], v[196:199], v[108:111]\n\t" \
    "global_load_dwordx4 v[164:167], v208, s[42:43] offset:3072\n\t" \
    "s_add_u32 s42, s42, 7168\n\t" \
    "s_addc_u32 s43, s43, 0\n\t" \
    "s_add_u32 s40, s40, 128\n\t" \
    "s_addc_u32 s41, s41, 0\n\t" \
    "s_waitcnt vmcnt(17)\n\t" \
    "s_waitcnt lgkmcnt(0)\n\t" \
    "v_mfma_f32_16x16x32_bf16 v[0:3], v[112:115], v[168:171], v[0:3]\n\t" \
    "v_mfma_f32_16x16x32_bf16 v[28:31], v[112:115], v[172:175], v[28:31]\n\t" \
    "ds_read_b128 v[184:187], v211 offset:16384\n\t" \
    "ds_read_b128 v[188:191], v211 offset:18432\n\t" \
    "ds_read_b128 v[192:195], v211 offset:20480\n\t" \
    "ds_read_b128 v[196:199], v211 offset:22528\n\t" \
    "v_mfma_f32_16x16x32_bf16 v[56:59], v[112:115], v[176:179], v[56:59]\n\t" \
    "v_mfma_f32_16x16x32_bf16 v[84:87], v[112:115], v[180:183], v[84:87]\n\t" \
    "global_load_dwordx4 v[112:115], v208, s[42:43] offset:-3072\n\t" \
    "v_mfma_f32_16x16x32_bf16 v[4:7], v[116:119], v[168:171], v[4:7]\n\t" \
    "v_mfma_f32_16x16x32_bf16 v[32:35], v[116:119], v[172:175], v[32:35]\n\t" \
    "s_add_u32 m0, %[ring], 8192\n\t" \
    "s_nop 0\n\t" \
    "global_load_lds_dwordx4 v200, s[40:41]\n\t" \
    "v_mfma_f32_16x16x32_bf16 v[60:63], v[116:119], v[176:179], v[60:63]\n\t" \
    "v_mfma_f32_16x16x32_bf16 v[88:91], v[116:119], v[180:183], v[88:91]\n\t" \
    "global_load_dwordx4 v[116:119], v208, s[42:43] offset:-2048\n\t" \
    "v_mfma_f32_16x16x32_bf16 v[8:11], v[120:123], v[168:171], v[8:11]\n\t" \
    "v_mfma_f32_16x16x32_bf16 v[36:39], v[120:123], v[172:175], v[36:39]\n\t" \
    "s_add_u32 m0, %[ring], 9216\n\t" \
    "s_nop 0\n\t" \
    "global_load_lds_dwordx4 v201, s[40:41]\n\t" \
    "v_mfma_f32_16x16x32_bf16 v[64:67], v[120:123], v[176:179], v[64:67]\n\t" \
    "v_mfma_f32_16x16x32_bf16 v[92:95], v[120:123], v[180:183], v[92:95]\n\t" \
    "global_load_dwordx4 v[120:123], v208, s[42:43] offset:-1024\n\t" \
    "s_waitcnt vmcnt(16)\n\t" \
    "v_mfma_f32_16x16x32_bf16 v[12:15], v[124:127], v[168:171], v[12:15]\n\t" \
    "v_mfma_f32_16x16x32_bf16 v[40:43], v[124:127], v[172:175], v[40:43]\n\t" \
    "s_add_u32 m0, %[ring], 10240\n\t" \
    "s_nop 0\n\t" \
    "global_load_lds_dwordx4 v202, s[40:41]\n\t" \
    "v_mfma_f32_16x16x32_bf16 v[68:71], v[124:127], v[176:179], v[68:71]\n\t" \
    "v_mfma_f32_16x16x32_bf16 v[96:99], v[124:127], v[180:183], v[96:99]\n\t" \
    "global_load_dwordx4 v[124:127], v208, s[42:43] offset:0\n\t" \
    "v_mfma_f32_16x16x32_bf16 v[16:19], v[128:131], v[168:171], v[16:19]\n\t" \
    "v_mfma_f32_16x16x32_bf16 v[44:47], v[128:131], v[172:175], v[44:47]\n\t" \
    "s_add_u32 m0, %[ring], 11264\n\t" \
    "s_nop 0\n\t" \
    "global_load_lds_dwordx4 v203, s[40:41]\n\t" \
    "v_mfma_f32_16x16x32_bf16 v[72:75], v[128:131], v[176:179], v[72:75]\n\t" \
    "v_mfma_f32_16x16x32_bf16 v[100:103], v[128:131], v[180:183], v[100:103]\n\t" \
    "global_load_dwordx4 v[128:131], v208, s[42:43] offset:1024\n\t" \
    "v_mfma_f32_16x16x32_bf16 v[20:23], v[132:135], v[168:171], v[20:23]\n\t" \
    "v_mfma_f32_16x16x32_bf16 v[48:51], v[132:135], v[172:175], v[48:51]\n\t" \
    "v_mfma_f32_16x16x32_bf16 v[76:79], v[132:135], v[176:179], v[76:79]\n\t" \
    "v_mfma_f32_16x16x32_bf16 v[104:107], v[132:135], v[180:183], v[104:107]\n\t" \
    "global_load_dwordx4 v[132:135], v208, s[42:43] offset:2048\n\t" \
    "v_mfma_f32_16x16x32_bf16 v[24:27], v[136:139], v[168:171], v[24:27]\n\t" \
    "v_mfma_f32_16x16x32_bf16 v[52:55], v[136:139], v[172:175], v[52:55]\n\t" \
    "v_mfma_f32_16x16x32_bf16 v[80:83], v[136:139], v[176:179], v[80:83]\n\t" \
    "v_mfma_f32_16x16x32_bf16 v[108:111], v[136:139], v[180:183], v[108:111]\n\t" \
    "global_load_dwordx4 v[136:139], v208, s[42:43] offset:3072\n\t" \
    "s_add_u32 s42, s42, 7168\n\t" \
    "s_addc_u32 s43, s43, 0\n\t" \
    "s_waitcnt vmcnt(14)\n\t" \
    "s_waitcnt lgkmcnt(0)\n\t" \
    "v_mfma_f32_16x16x32_bf16 v[0:3], v[140:143], v[184:187], v[0:3]\n\t" \
    "v_mfma_f32_16x16x32_bf16 v[28:31], v[140:143], v[188:191], v[28:31]\n\t" \
    "ds_read_b128 v[168:171], v210 offset:0\n\t" \
    "ds_read_b128 v[172:175], v210 offset:2048\n\t" \
    "ds_read_b128 v[176:179], v210 offset:4096\n\t" \
    "ds_read_b128 v[180:183], v210 offset:6144\n\t" \
    "v_mfma_f32_16x16x32_bf16 v[56:59], v[140:143], v[192:195], v[56:59]\n\t" \
    "v_mfma_f32_16x16x32_bf16 v[84:87], v[140:143], v[196:199], v[84:87]\n\t" \
    "global_load_dwordx4 v[140:143], v208, s[42:43] offset:-3072\n\t" \
    "v_mfma_f32_16x16x32_bf16 v[4:7], v[144:147], v[184:187], v[4:7]\n\t" \
    "v_mfma_f32_16x16x32_bf16 v[32:35], v[144:147], v[188:191], v[32:35]\n\t" \
    "s_add_u32 m0, %[ring], 12288\n\t" \
    "s_nop 0\n\t" \
    "global_load_lds_dwordx4 v204, s[40:41]\n\t" \
    "v_mfma_f32_16x16x32_bf16 v[60:63], v[144:147], v[192:195], v[60:63]\n\t" \
    "v_mfma_f32_16x16x32_bf16 v[88:91], v[144:147], v[196:199], v[88:91]\n\t" \
    "global_load_dwordx4 v[144:147], v208, s[42:43] offset:-2048\n\t" \
    "v_mfma_f32_16x16x32_bf16 v[8:11], v[148:151], v[184:187], v[8:11]\n\t" \
    "v_mfma_f32_16x16x32_bf16 v[36:39], v[148:151], v[188:191], v[36:39]\n\t" \
    "s_add_u32 m0, %[ring], 13312\n\t" \
    "s_nop 0\n\t" \
    "global_load_lds_dwordx4 v205, s[40:41]\n\t" \
    "v_mfma_f32_16x16x32_bf16 v[64:67], v[148:151], v[192:195], v[64:67]\n\t" \
    "v_mfma_f32_16x16x32_bf16 v[92:95], v[148:151], v[196:199], v[92:95]\n\t" \
    "global_load_dwordx4 v[148:151], v208, s[42:43] offset:-1024\n\t" \
    "s_waitcnt vmcnt(16)\n\t" \
    "v_mfma_f32_16x16x32_bf16 v[12:15], v[152:155], v[184:187], v[12:15]\n\t" \
    "v_mfma_f32_16x16x32_bf16 v[40:43], v[152:155], v[188:191], v[40:43]\n\t" \
    "s_add_u32 m0, %[ring], 14336\n\t" \
    "s_nop 0\n\t" \
    "global_load_lds_dwordx4 v206, s[40:41]\n\t" \
    "v_mfma_f32_16x16x32_bf16 v[68:71], v[152:155], v[192:195], v[68:71]\n\t" \
    "v_mfma_f32_16x16x32_bf16 v[96:99], v[152:155], v[196:199], v[96:99]\n\t" \
    "global_load_dwordx4 v[152:155], v208, s[42:43] offset:0\n\t" \
    "v_mfma_f32_16x16x32_bf16 v[16:19], v[156:159], v[184:187], v[16:19]\n\t" \
    "v_mfma_f32_16x16x32_bf16 v[44:47], v[156:159], v[188:191], v[44:47]\n\t" \
    "s_add_u32 m0, %[ring], 15360\n\t" \
    "s_nop 0\n\t" \
    "global_load_lds_dwordx4 v207, s[40:41]\n\t" \
    "v_mfma_f32_16x16x32_bf16 v[72:75], v[156:159], v[192:195], v[72:75]\n\t" \
    "v_mfma_f32_16x16x32_bf16 v[100:103], v[156:159], v[196:199], v[100:103]\n\t" \
    "global_load_dwordx4 v[156:159], v208, s[42:43] offset:1024\n\t" \
    "v_mfma_f32_16x16x32_bf16 v[20:23], v[160:163], v[184:187], v[20:23]\n\t" \
    "v_mfma_f32_16x16x32_bf16 v[48:51], v[160:163], v[188:191], v[48:51]\n\t" \
    "v_mfma_f32_16x16x32_bf16 v[76:79], v[160:163], v[192:195], v[76:79]\n\t" \
    "v_mfma_f32_16x16x32_bf16 v[104:107], v[160:163], v[196:199], v[104:107]\n\t" \
    "global_load_dwordx4 v[160:163], v208, s[42:43] offset:2048\n\t" \
    "v_mfma_f32_16x16x32_bf16 v[24:27], v[164:167], v[184:187], v[24:27]\n\t" \
    "v_mfma_f32_16x16x32_bf16 v[52:55], v[164:167], v[188:191], v[52:55]\n\t" \
    "v_mfma_f32_16x16x32_bf16 v[80:83], v[164:167], v[192:195], v[80:83]\n\t" \
    "v_mfma_f32_16x16x32_bf16 v[108:111], v[164:167], v[196:199], v[108:111]\n\t" \
    "global_load_dwordx4 v[164:167], v208, s[42:43] offset:3072\n\t" \
    "s_add_u32 s42, s42, 7168\n\t" \
    "s_addc_u32 s43, s43, 0\n\t" \
    "s_add_u32 s40, s40, 128\n\t" \
    "s_addc_u32 s41, s41, 0\n\t" \
    "s_waitcnt vmcnt(17)\n\t" \
    "s_waitcnt lgkmcnt(0)\n\t" \
    "v_mfma_f32_16x16x32_bf16 v[0:3], v[112:115], v[168:171], v[0:3]\n\t" \
    "v_mfma_f32_16x16x32_bf16 v[28:31], v[112:115], v[172:175], v[28:31]\n\t" \
    "ds_read_b128 v[184:187], v211 offset:0\n\t" \
    "ds_read_b128 v[188:191], v211 offset:2048\n\t" \
    "ds_read_b128 v[192:195], v211 offset:4096\n\t" \
    "ds_read_b128 v[196:199], v211 offset:6144\n\t" \
    "v_mfma_f32_16x16x32_bf16 v[56:59], v[112:115], v[176:179], v[56:59]\n\t" \
    "v_mfma_f32_16x16x32_bf16 v[84:87], v[112:115], v[180:183], v[84:87]\n\t" \
    "global_load_dwordx4 v[112:115], v208, s[42:43] offset:-3072\n\t" \
    "v_mfma_f32_16x16x32_bf16 v[4:7], v[116:119], v[168:171], v[4:7]\n\t" \
    "v_mfma_f32_16x16x32_bf16 v[32:35], v[116:119], v[172:175], v[32:35]\n\t" \
    "s_add_u32 m0, %[ring], 16384\n\t" \
    "s_nop 0\n\t" \
    "global_load_lds_dwordx4 v200, s[40:41]\n\t" \
    "v_mfma_f32_16x16x32_bf16 v[60:63], v[116:119], v[176:179], v[60:63]\n\t" \
    "v_mfma_f32_16x16x32_bf16 v[88:91], v[116:119], v[180:183], v[88:91]\n\t" \
    "global_load_dwordx4 v[116:119], v208, s[42:43] offset:-2048\n\t" \
    "v_mfma_f32_16x16x32_bf16 v[8:11], v[120:123], v[168:171], v[8:11]\n\t" \
    "v_mfma_f32_16x16x32_bf16 v[36:39], v[120:123], v[172:175], v[36:39]\n\t" \
    "s_add_u32 m0, %[ring], 17408\n\t" \
    "s_nop 0\n\t" \
    "global_load_lds_dwordx4 v201, s[40:41]\n\t" \
    "v_mfma_f32_16x16x32_bf16 v[64:67], v[120:123], v[176:179], v[64:67]\n\t" \
    "v_mfma_f32_16x16x32_bf16 v[92:95], v[120:123], v[180:183], v[92:95]\n\t" \
    "global_load_dwordx4 v[120:123], v208, s[42:43] offset:-1024\n\t" \
    "s_waitcnt vmcnt(16)\n\t" \
    "v_mfma_f32_16x16x32_bf16 v[12:15], v[124:127], v[168:171], v[12:15]\n\t" \
    "v_mfma_f32_16x16x32_bf16 v[40:43], v[124:127], v[172:175], v[40:43]\n\t" \
    "s_add_u32 m0, %[ring], 18432\n\t" \
    "s_nop 0\n\t" \
    "global_load_lds_dwordx4 v202, s[40:41]\n\t" \
    "v_mfma_f32_16x16x32_bf16 v[68:71], v[124:127], v[176:179], v[68:71]\n\t" \
    "v_mfma_f32_16x16x32_bf16 v[96:99], v[124:127], v[180:183], v[96:99]\n\t" \
    "global_load_dwordx4 v[124:127], v208, s[42:43] offset:0\n\t" \
    "v_mfma_f32_16x16x32_bf16 v[16:19], v[128:131], v[168:171], v[16:19]\n\t" \
    "v_mfma_f32_16x16x32_bf16 v[44:47], v[128:131], v[172:175], v[44:47]\n\t" \
    "s_add_u32 m0, %[ring], 19456\n\t" \
    "s_nop 0\n\t" \
    "global_load_lds_dwordx4 v203, s[40:41]\n\t" \
    "v_mfma_f32_16x16x32_bf16 v[72:75], v[128:131], v[176:179], v[72:75]\n\t" \
    "v_mfma_f32_16x16x32_bf16 v[100:103], v[128:131], v[180:183], v[100:103]\n\t" \
    "global_load_dwordx4 v[128:131], v208, s[42:43] offset:1024\n\t" \
    "v_mfma_f32_16x16x32_bf16 v[20:23], v[132:135], v[168:171], v[20:23]\n\t" \
    "v_mfma_f32_16x16x32_bf16 v[48:51], v[132:135], v[172:175], v[48:51]\n\t" \
    "v_mfma_f32_16x16x32_bf16 v[76:79], v[132:135], v[176:179], v[76:79]\n\t" \
    "v_mfma_f32_16x16x32_bf16 v[104:107], v[132:135], v[180:183], v[104:107]\n\t" \
    "global_load_dwordx4 v[132:135], v208, s[42:43] offset:2048\n\t" \
    "v_mfma_f32_16x16x32_bf16 v[24:27], v[136:139], v[168:171], v[24:27]\n\t" \
    "v_mfma_f32_16x16x32_bf16 v[52:55], v[136:139], v[172:175], v[52:55]\n\t" \
    "v_mfma_f32_16x16x32_bf16 v[80:83], v[136:139], v[176:179], v[80:83]\n\t" \
    "v_mfma_f32_16x16x32_bf16 v[108:111], v[136:139], v[180:183], v[108:111]\n\t" \
    "global_load_dwordx4 v[136:139], v208, s[42:43] offset:3072\n\t" \
    "s_add_u32 s42, s42, 7168\n\t" \
    "s_addc_u32 s43, s43, 0\n\t" \
    "s_waitcnt vmcnt(14)\n\t" \
    "s_waitcnt lgkmcnt(0)\n\t" \
    "v_mfma_f32_16x16x32_bf16 v[0:3], v[140:143], v[184:187], v[0:3]\n\t" \
    "v_mfma_f32_16x16x32_bf16 v[28:31], v[140:143], v[188:191], v[28:31]\n\t" \
    "ds_read_b128 v[168:171], v210 offset:8192\n\t" \
    "ds_read_b128 v[172:175], v210 offset:10240\n\t" \
    "ds_read_b128 v[176:179], v210 offset:12288\n\t" \
    "ds_read_b128 v[180:183], v210 offset:14336\n\t" \
    "v_mfma_f32_16x16x32_bf16 v[56:59], v[140:143], v[192:195], v[56:59]\n\t" \
    "v_mfma_f32_16x16x32_bf16 v[84:87], v[140:143], v[196:199], v[84:87]\n\t" \
    "global_load_dwordx4 v[140:143], v208, s[42:43] offset:-3072\n\t" \
    "v_mfma_f32_16x16x32_bf16 v[4:7], v[144:147], v[184:187], v[4:7]\n\t" \
    "v_mfma_f32_16x16x32_bf16 v[32:35], v[144:147], v[188:191], v[32:35]\n\t" \
    "s_add_u32 m0, %[ring], 20480\n\t" \
    "s_nop 0\n\t" \
    "global_load_lds_dwordx4 v204, s[40:41]\n\t" \
    "v_mfma_f32_16x16x32_bf16 v[60:63], v[144:147], v[192:195], v[60:63]\n\t" \
    "v_mfma_f32_16x16x32_bf16 v[88:91], v[144:147], v[196:199], v[88:91]\n\t" \
    "global_load_dwordx4 v[144:147], v208, s[42:43] offset:-2048\n\t" \
    "v_mfma_f32_16x16x32_bf16 v[8:11], v[148:151], v[184:187], v[8:11]\n\t" \
    "v_mfma_f32_16x16x32_bf16 v[36:39], v[148:151], v[188:191], v[36:39]\n\t" \
    "s_add_u32 m0, %[ring], 21504\n\t" \
    "s_nop 0\n\t" \
    "global_load_lds_dwordx4 v205, s[40:41]\n\t" \
    "v_mfma_f32_16x16x32_bf16 v[64:67], v[148:151], v[192:195], v[64:67]\n\t" \
    "v_mfma_f32_16x16x32_bf16 v[92:95], v[148:151], v[196:199], v[92:95]\n\t" \
    "global_load_dwordx4 v[148:151], v208, s[42:43] offset:-1024\n\t" \
    "s_waitcnt vmcnt(16)\n\t" \
    "v_mfma_f32_16x16x32_bf16 v[12:15], v[152:155], v[184:187], v[12:15]\n\t" \
    "v_mfma_f32_16x16x32_bf16 v[40:43], v[152:155], v[188:191], v[40:43]\n\t" \
    "s_add_u32 m0, %[ring], 22528\n\t" \
    "s_nop 0\n\t" \
    "global_load_lds_dwordx4 v206, s[40:41]\n\t" \
    "v_mfma_f32_16x16x32_bf16 v[68:71], v[152:155], v[192:195], v[68:71]\n\t" \
    "v_mfma_f32_16x16x32_bf16 v[96:99], v[152:155], v[196:199], v[96:99]\n\t" \
    "global_load_dwordx4 v[152:155], v208, s[42:43] offset:0\n\t" \
    "v_mfma_f32_16x16x32_bf16 v[16:19], v[156:159], v[184:187], v[16:19]\n\t" \
    "v_mfma_f32_16x16x32_bf16 v[44:47], v[156:159], v[188:191], v[44:47]\n\t" \
    "s_add_u32 m0, %[ring], 23552\n\t" \
    "s_nop 0\n\t" \
    "global_load_lds_dwordx4 v207, s[40:41]\n\t" \
    "v_mfma_f32_16x16x32_bf16 v[72:75], v[156:159], v[192:195], v[72:75]\n\t" \
    "v_mfma_f32_16x16x32_bf16 v[100:103], v[156:159], v[196:199], v[100:103]\n\t" \
    "global_load_dwordx4 v[156:159], v208, s[42:43] offset:1024\n\t" \
    "v_mfma_f32_16x16x32_bf16 v[20:23], v[160:163], v[184:187], v[20:23]\n\t" \
    "v_mfma_f32_16x16x32_bf16 v[48:51], v[160:163], v[188:191], v[48:51]\n\t" \
    "v_mfma_f32_16x16x32_bf16 v[76:79], v[160:163], v[192:195], v[76:79]\n\t" \
    "v_mfma_f32_16x16x32_bf16 v[104:107], v[160:163], v[196:199], v[104:107]\n\t" \
    "global_load_dwordx4 v[160:163], v208, s[42:43] offset:2048\n\t" \
    "v_mfma_f32_16x16x32_bf16 v[24:27], v[164:167], v[184:187], v[24:27]\n\t" \
    "v_mfma_f32_16x16x32_bf16 v[52:55], v[164:167], v[188:191], v[52:55]\n\t" \
    "v_mfma_f32_16x16x32_bf16 v[80:83], v[164:167], v[192:195], v[80:83]\n\t" \
    "v_mfma_f32_16x16x32_bf16 v[108:111], v[164:167], v[196:199], v[108:111]\n\t" \
    "global_load_dwordx4 v[164:167], v208, s[42:43] offset:3072\n\t" \
    "s_add_u32 s42, s42, 7168\n\t" \
    "s_addc_u32 s43, s43, 0\n\t" \
    "s_add_u32 s40, s40, 128\n\t" \
    "s_addc_u32 s41, s41, 0\n\t" \
    "s_waitcnt vmcnt(17)\n\t" \
    "s_waitcnt lgkmcnt(0)\n\t" \
    "v_mfma_f32_16x16x32_bf16 v[0:3], v[112:115], v[168:171], v[0:3]\n\t" \
    "v_mfma_f32_16x16x32_bf16 v[28:31], v[112:115], v[172:175], v[28:31]\n\t" \
    "ds_read_b128 v[184:187], v211 offset:8192\n\t" \
    "ds_read_b128 v[188:191], v211 offset:10240\n\t" \
    "ds_read_b128 v[192:195], v211 offset:12288\n\t" \
    "ds_read_b128 v[196:199], v211 offset:14336\n\t" \
    "v_mfma_f32_16x16x32_bf16 v[56:59], v[112:115], v[176:179], v[56:59]\n\t" \
    "v_mfma_f32_16x16x32_bf16 v[84:87], v[112:115], v[180:183], v[84:87]\n\t" \
    "global_load_dwordx4 v[112:115], v208, s[42:43] offset:-3072\n\t" \
    "v_mfma_f32_16x16x32_bf16 v[4:7], v[116:119], v[168:171], v[4:7]\n\t" \
    "v_mfma_f32_16x16x32_bf16 v[32:35], v[116:119], v[172:175], v[32:35]\n\t" \
    "s_add_u32 m0, %[ring], 0\n\t" \
    "s_nop 0\n\t" \
    "global_load_lds_dwordx4 v200, s[40:41]\n\t" \
    "v_mfma_f32_16x16x32_bf16 v[60:63], v[116:119], v[176:179], v[60:63]\n\t" \
    "v_mfma_f32_16x16x32_bf16 v[88:91], v[116:119], v[180:183], v[88:91]\n\t" \
    "global_load_dwordx4 v[116:119], v208, s[42:43] offset:-2048\n\t" \
    "v_mfma_f32_16x16x32_bf16 v[8:11], v[120:123], v[168:171], v[8:11]\n\t" \
    "v_mfma_f32_16x16x32_bf16 v[36:39], v[120:123], v[172:175], v[36:39]\n\t" \
    "s_add_u32 m0, %[ring], 1024\n\t" \
    "s_nop 0\n\t" \
    "global_load_lds_dwordx4 v201, s[40:41]\n\t" \
    "v_mfma_f32_16x16x32_bf16 v[64:67], v[120:123], v[176:179], v[64:67]\n\t" \
    "v_mfma_f32_16x16x32_bf16 v[92:95], v[120:123], v[180:183], v[92:95]\n\t" \
    "global_load_dwordx4 v[120:123], v208, s[42:43] offset:-1024\n\t" \
    "s_waitcnt vmcnt(16)\n\t" \
    "v_mfma_f32_16x16x32_bf16 v[12:15], v[124:127], v[168:171], v[12:15]\n\t" \
    "v_mfma_f32_16x16x32_bf16 v[40:43], v[124:127], v[172:175], v[40:43]\n\t" \
    "s_add_u32 m0, %[ring], 2048\n\t" \
    "s_nop 0\n\t" \
    "global_load_lds_dwordx4 v202, s[40:41]\n\t" \
    "v_mfma_f32_16x16x32_bf16 v[68:71], v[124:127], v[176:179], v[68:71]\n\t" \
    "v_mfma_f32_16x16x32_bf16 v[96:99], v[124:127], v[180:183], v[96:99]\n\t" \
    "global_load_dwordx4 v[124:127], v208, s[42:43] offset:0\n\t" \
    "v_mfma_f32_16x16x32_bf16 v[16:19], v[128:131], v[168:171], v[16:19]\n\t" \
    "v_mfma_f32_16x16x32_bf16 v[44:47], v[128:131], v[172:175], v[44:47]\n\t" \
    "s_add_u32 m0, %[ring], 3072\n\t" \
    "s_nop 0\n\t" \
    "global_load_lds_dwordx4 v203, s[40:41]\n\t" \
    "v_mfma_f32_16x16x32_bf16 v[72:75], v[128:131], v[176:179], v[72:75]\n\t" \
    "v_mfma_f32_16x16x32_bf16 v[100:103], v[128:131], v[180:183], v[100:103]\n\t" \
    "global_load_dwordx4 v[128:131], v208, s[42:43] offset:1024\n\t" \
    "v_mfma_f32_16x16x32_bf16 v[20:23], v[132:135], v[168:171], v[20:23]\n\t" \
    "v_mfma_f32_16x16x32_bf16 v[48:51], v[132:135], v[172:175], v[48:51]\n\t" \
    "v_mfma_f32_16x16x32_bf16 v[76:79], v[132:135], v[176:179], v[76:79]\n\t" \
    "v_mfma_f32_16x16x32_bf16 v[104:107], v[132:135], v[180:183], v[104:107]\n\t" \
    "global_load_dwordx4 v[132:135], v208, s[42:43] offset:2048\n\t" \
    "v_mfma_f32_16x16x32_bf16 v[24:27], v[136:139], v[168:171], v[24:27]\n\t" \
    "v_mfma_f32_16x16x32_bf16 v[52:55], v[136:139], v[172:175], v[52:55]\n\t" \
    "v_mfma_f32_16x16x32_bf16 v[80:83], v[136:139], v[176:179], v[80:83]\n\t" \
    "v_mfma_f32_16x16x32_bf16 v[108:111], v[136:139], v[180:183], v[108:111]\n\t" \
    "global_load_dwordx4 v[136:139], v208, s[42:43] offset:3072\n\t" \
    "s_add_u32 s42, s42, 7168\n\t" \
    "s_addc_u32 s43, s43, 0\n\t" \
    "s_waitcnt vmcnt(14)\n\t" \
    "s_waitcnt lgkmcnt(0)\n\t" \
    "v_mfma_f32_16x16x32_bf16 v[0:3], v[140:143], v[184:187], v[0:3]\n\t" \
    "v_mfma_f32_16x16x32_bf16 v[28:31], v[140:143], v[188:191], v[28:31]\n\t" \
    "ds_read_b128 v[168:171], v210 offset:16384\n\t" \
    "ds_read_b128 v[172:175], v210 offset:18432\n\t" \
    "ds_read_b128 v[176:179], v210 offset:20480\n\t" \
    "ds_read_b128 v[180:183], v210 offset:22528\n\t" \
    "v_mfma_f32_16x16x32_bf16 v[56:59], v[140:143], v[192:195], v[56:59]\n\t" \
    "v_mfma_f32_16x16x32_bf16 v[84:87], v[140:143], v[196:199], v[84:87]\n\t" \
    "global_load_dwordx4 v[140:143], v208, s[42:43] offset:-3072\n\t" \
    "v_mfma_f32_16x16x32_bf16 v[4:7], v[144:147], v[184:187], v[4:7]\n\t" \
    "v_mfma_f32_16x16x32_bf16 v[32:35], v[144:147], v[188:191], v[32:35]\n\t" \
    "s_add_u32 m0, %[ring], 4096\n\t" \
    "s_nop 0\n\t" \
    "global_load_lds_dwordx4 v204, s[40:41]\n\t" \
    "v_mfma_f32_16x16x32_bf16 v[60:63], v[144:147], v[192:195], v[60:63]\n\t" \
    "v_mfma_f32_16x16x32_bf16 v[88:91], v[144:147], v[196:199], v[88:91]\n\t" \
    "global_load_dwordx4 v[144:147], v208, s[42:43] offset:-2048\n\t" \
    "v_mfma_f32_16x16x32_bf16 v[8:11], v[148:151], v[184:187], v[8:11]\n\t" \
    "v_mfma_f32_16x16x32_bf16 v[36:39], v[148:151], v[188:191], v[36:39]\n\t" \
    "s_add_u32 m0, %[ring], 5120\n\t" \
    "s_nop 0\n\t" \
    "global_load_lds_dwordx4 v205, s[40:41]\n\t" \
    "v_mfma_f32_16x16x32_bf16 v[64:67], v[148:151], v[192:195], v[64:67]\n\t" \
    "v_mfma_f32_16x16x32_bf16 v[92:95], v[148:151], v[196:199], v[92:95]\n\t" \
    "global_load_dwordx4 v[148:151], v208, s[42:43] offset:-1024\n\t" \
    "s_waitcnt vmcnt(16)\n\t" \
    "v_mfma_f32_16x16x32_bf16 v[12:15], v[152:155], v[184:187], v[12:15]\n\t" \
    "v_mfma_f32_16x16x32_bf16 v[40:43], v[152:155], v[188:191], v[40:43]\n\t" \
    "s_add_u32 m0, %[ring], 6144\n\t" \
    "s_nop 0\n\t" \
    "global_load_lds_dwordx4 v206, s[40:41]\n\t" \
    "v_mfma_f32_16x16x32_bf16 v[68:71], v[152:155], v[192:195], v[68:71]\n\t" \
    "v_mfma_f32_16x16x32_bf16 v[96:99], v[152:155], v[196:199], v[96:99]\n\t" \
    "global_load_dwordx4 v[152:155], v208, s[42:43] offset:0\n\t" \
    "v_mfma_f32_16x16x32_bf16 v[16:19], v[156:159], v[184:187], v[16:19]\n\t" \
    "v_mfma_f32_16x16x32_bf16 v[44:47], v[156:159], v[188:191], v[44:47]\n\t" \
    "s_add_u32 m0, %[ring], 7168\n\t" \
    "s_nop 0\n\t" \
    "global_load_lds_dwordx4 v207, s[40:41]\n\t" \
    "v_mfma_f32_16x16x32_bf16 v[72:75], v[156:159], v[192:195], v[72:75]\n\t" \
    "v_mfma_f32_16x16x32_bf16 v[100:103], v[156:159], v[196:199], v[100:103]\n\t" \
    "global_load_dwordx4 v[156:159], v208, s[42:43] offset:1024\n\t" \
    "v_mfma_f32_16x16x32_bf16 v[20:23], v[160:163], v[184:187], v[20:23]\n\t" \
    "v_mfma_f32_16x16x32_bf16 v[48:51], v[160:163], v[188:191], v[48:51]\n\t" \
    "v_mfma_f32_16x16x32_bf16 v[76:79], v[160:163], v[192:195], v[76:79]\n\t" \
    "v_mfma_f32_16x16x32_bf16 v[104:107], v[160:163], v[196:199], v[104:107]\n\t" \
    "global_load_dwordx4 v[160:163], v208, s[42:43] offset:2048\n\t" \
    "v_mfma_f32_16x16x32_bf16 v[24:27], v[164:167], v[184:187], v[24:27]\n\t" \
    "v_mfma_f32_16x16x32_bf16 v[52:55], v[164:167], v[188:191], v[52:55]\n\t" \
    "v_mfma_f32_16x16x32_bf16 v[80:83], v[164:167], v[192:195], v[80:83]\n\t" \
    "v_mfma_f32_16x16x32_bf16 v[108:111], v[164:167], v[196:199], v[108:111]\n\t" \
    "global_load_dwordx4 v[164:167], v208, s[42:43] offset:3072\n\t" \
    "s_add_u32 s42, s42, 7168\n\t" \
    "s_addc_u32 s43, s43, 0\n\t" \
    "s_add_u32 s40, s40, 128\n\t" \
    "s_addc_u32 s41, s41, 0\n\t" \
    "s_waitcnt vmcnt(17)\n\t" \
    "s_waitcnt lgkmcnt(0)\n\t" \
    "v_mfma_f32_16x16x32_bf16 v[0:3], v[112:115], v[168:171], v[0:3]\n\t" \
    "v_mfma_f32_16x16x32_bf16 v[28:31], v[112:115], v[172:175], v[28:31]\n\t" \
    "ds_read_b128 v[184:187], v211 offset:16384\n\t" \
    "ds_read_b128 v[188:191], v211 offset:18432\n\t" \
    "ds_read_b128 v[192:195], v211 offset:20480\n\t" \
    "ds_read_b128 v[196:199], v211 offset:22528\n\t" \
    "v_mfma_f32_16x16x32_bf16 v[56:59], v[112:115], v[176:179], v[56:59]\n\t" \
    "v_mfma_f32_16x16x32_bf16 v[84:87], v[112:115], v[180:183], v[84:87]\n\t" \
    "global_load_dwordx4 v[112:115], v208, s[42:43] offset:-3072\n\t" \
    "v_mfma_f32_16x16x32_bf16 v[4:7], v[116:119], v[168:171], v[4:7]\n\t" \
    "v_mfma_f32_16x16x32_bf16 v[32:35], v[116:119], v[172:175], v[32:35]\n\t" \
    "s_add_u32 m0, %[ring], 8192\n\t" \
    "s_nop 0\n\t" \
    "global_load_lds_dwordx4 v200, s[40:41]\n\t" \
    "v_mfma_f32_16x16x32_bf16 v[60:63], v[116:119], v[176:179], v[60:63]\n\t" \
    "v_mfma_f32_16x16x32_bf16 v[88:91], v[116:119], v[180:183], v[88:91]\n\t" \
    "global_load_dwordx4 v[116:119], v208, s[42:43] offset:-2048\n\t" \
    "v_mfma_f32_16x16x32_bf16 v[8:11], v[120:123], v[168:171], v[8:11]\n\t" \
    "v_mfma_f32_16x16x32_bf16 v[36:39], v[120:123], v[172:175], v[36:39]\n\t" \
    "s_add_u32 m0, %[ring], 9216\n\t" \
    "s_nop 0\n\t" \
    "global_load_lds_dwordx4 v201, s[40:41]\n\t" \
    "v_mfma_f32_16x16x32_bf16 v[64:67], v[120:123], v[176:179], v[64:67]\n\t" \
    "v_mfma_f32_16x16x32_bf16 v[92:95], v[120:123], v[180:183], v[92:95]\n\t" \
    "global_load_dwordx4 v[120:123], v208, s[42:43] offset:-1024\n\t" \
    "s_waitcnt vmcnt(16)\n\t" \
    "v_mfma_f32_16x16x32_bf16 v[12:15], v[124:127], v[168:171], v[12:15]\n\t" \
    "v_mfma_f32_16x16x32_bf16 v[40:43], v[124:127], v[172:175], v[40:43]\n\t" \
    "s_add_u32 m0, %[ring], 10240\n\t" \
    "s_nop 0\n\t" \
    "global_load_lds_dwordx4 v202, s[40:41]\n\t" \
    "v_mfma_f32_16x16x32_bf16 v[68:71], v[124:127], v[176:179], v[68:71]\n\t" \
    "v_mfma_f32_16x16x32_bf16 v[96:99], v[124:127], v[180:183], v[96:99]\n\t" \
    "global_load_dwordx4 v[124:127], v208, s[42:43] offset:0\n\t" \
    "v_mfma_f32_16x16x32_bf16 v[16:19], v[128:131], v[168:171], v[16:19]\n\t" \
    "v_mfma_f32_16x16x32_bf16 v[44:47], v[128:131], v[172:175], v[44:47]\n\t" \
    "s_add_u32 m0, %[ring], 11264\n\t" \
    "s_nop 0\n\t" \
    "global_load_lds_dwordx4 v203, s[40:41]\n\t" \
    "v_mfma_f32_16x16x32_bf16 v[72:75], v[128:131], v[176:179], v[72:75]\n\t" \
    "v_mfma_f32_16x16x32_bf16 v[100:103], v[128:131], v[180:183], v[100:103]\n\t" \
    "global_load_dwordx4 v[128:131], v208, s[42:43] offset:1024\n\t" \
    "v_mfma_f32_16x16x32_bf16 v[20:23], v[132:135], v[168:171], v[20:23]\n\t" \
    "v_mfma_f32_16x16x32_bf16 v[48:51], v[132:135], v[172:175], v[48:51]\n\t" \
    "v_mfma_f32_16x16x32_bf16 v[76:79], v[132:135], v[176:179], v[76:79]\n\t" \
    "v_mfma_f32_16x16x32_bf16 v[104:107], v[132:135], v[180:183], v[104:107]\n\t" \
    "global_load_dwordx4 v[132:135], v208, s[42:43] offset:2048\n\t" \
    "v_mfma_f32_16x16x32_bf16 v[24:27], v[136:139], v[168:171], v[24:27]\n\t" \
    "v_mfma_f32_16x16x32_bf16 v[52:55], v[136:139], v[172:175], v[52:55]\n\t" \
    "v_mfma_f32_16x16x32_bf16 v[80:83], v[136:139], v[176:179], v[80:83]\n\t" \
    "v_mfma_f32_16x16x32_bf16 v[108:111], v[136:139], v[180:183], v[108:111]\n\t" \
    "global_load_dwordx4 v[136:139], v208, s[42:43] offset:3072\n\t" \
    "s_add_u32 s42, s42, 7168\n\t" \
    "s_addc_u32 s43, s43, 0\n\t" \
    "s_waitcnt vmcnt(14)\n\t" \
    "s_waitcnt lgkmcnt(0)\n\t" \
    "v_mfma_f32_16x16x32_bf16 v[0:3], v[140:143], v[184:187], v[0:3]\n\t" \
    "v_mfma_f32_16x16x32_bf16 v[28:31], v[140:143], v[188:191], v[28:31]\n\t" \
    "ds_read_b128 v[168:171], v210 offset:0\n\t" \
    "ds_read_b128 v[172:175], v210 offset:2048\n\t" \
    "ds_read_b128 v[176:179], v210 offset:4096\n\t" \
    "ds_read_b128 v[180:183], v210 offset:6144\n\t" \
    "v_mfma_f32_16x16x32_bf16 v[56:59], v[140:143], v[192:195], v[56:59]\n\t" \
    "v_mfma_f32_16x16x32_bf16 v[84:87], v[140:143], v[196:199], v[84:87]\n\t" \
    "global_load_dwordx4 v[140:143], v208, s[42:43] offset:-3072\n\t" \
    "v_mfma_f32_16x16x32_bf16 v[4:7], v[144:147], v[184:187], v[4:7]\n\t" \
    "v_mfma_f32_16x16x32_bf16 v[32:35], v[144:147], v[188:191], v[32:35]\n\t" \
    "s_add_u32 m0, %[ring], 12288\n\t" \
    "s_nop 0\n\t" \
    "global_load_lds_dwordx4 v204, s[40:41]\n\t" \
    "v_mfma_f32_16x16x32_bf16 v[60:63], v[144:147], v[192:195], v[60:63]\n\t" \
    "v_mfma_f32_16x16x32_bf16 v[88:91], v[144:147], v[196:199], v[88:91]\n\t" \
    "global_load_dwordx4 v[144:147], v208, s[42:43] offset:-2048\n\t" \
    "v_mfma_f32_16x16x32_bf16 v[8:11], v[148:151], v[184:187], v[8:11]\n\t" \
    "v_mfma_f32_16x16x32_bf16 v[36:39], v[148:151], v[188:191], v[36:39]\n\t" \
    "s_add_u32 m0, %[ring], 13312\n\t" \
    "s_nop 0\n\t" \
    "global_load_lds_dwordx4 v205, s[40:41]\n\t" \
    "v_mfma_f32_16x16x32_bf16 v[64:67], v[148:151], v[192:195], v[64:67]\n\t" \
    "v_mfma_f32_16x16x32_bf16 v[92:95], v[148:151], v[196:199], v[92:95]\n\t" \
    "global_load_dwordx4 v[148:151], v208, s[42:43] offset:-1024\n\t" \
    "s_waitcnt vmcnt(16)\n\t" \
    "v_mfma_f32_16x16x32_bf16 v[12:15], v[152:155], v[184:187], v[12:15]\n\t" \
    "v_mfma_f32_16x16x32_bf16 v[40:43], v[152:155], v[188:191], v[40:43]\n\t" \
    "s_add_u32 m0, %[ring], 14336\n\t" \
    "s_nop 0\n\t" \
    "global_load_lds_dwordx4 v206, s[40:41]\n\t" \
    "v_mfma_f32_16x16x32_bf16 v[68:71], v[152:155], v[192:195], v[68:71]\n\t" \
    "v_mfma_f32_16x16x32_bf16 v[96:99], v[152:155], v[196:199], v[96:99]\n\t" \
    "global_load_dwordx4 v[152:155], v208, s[42:43] offset:0\n\t" \
    "v_mfma_f32_16x16x32_bf16 v[16:19], v[156:159], v[184:187], v[16:19]\n\t" \
    "v_mfma_f32_16x16x32_bf16 v[44:47], v[156:159], v[188:191], v[44:47]\n\t" \
    "s_add_u32 m0, %[ring], 15360\n\t" \
    "s_nop 0\n\t" \
    "global_load_lds_dwordx4 v207, s[40:41]\n\t" \
    "v_mfma_f32_16x16x32_bf16 v[72:75], v[156:159], v[192:195], v[72:75]\n\t" \
    "v_mfma_f32_16x16x32_bf16 v[100:103], v[156:159], v[196:199], v[100:103]\n\t" \
    "global_load_dwordx4 v[156:159], v208, s[42:43] offset:1024\n\t" \
    "v_mfma_f32_16x16x32_bf16 v[20:23], v[160:163], v[184:187], v[20:23]\n\t" \
    "v_mfma_f32_16x16x32_bf16 v[48:51], v[160:163], v[188:191], v[48:51]\n\t" \
    "v_mfma_f32_16x16x32_bf16 v[76:79], v[160:163], v[192:195], v[76:79]\n\t" \
    "v_mfma_f32_16x16x32_bf16 v[104:107], v[160:163], v[196:199], v[104:107]\n\t" \
    "global_load_dwordx4 v[160:163], v208, s[42:43] offset:2048\n\t" \
    "v_mfma_f32_16x16x32_bf16 v[24:27], v[164:167], v[184:187], v[24:27]\n\t" \
    "v_mfma_f32_16x16x32_bf16 v[52:55], v[164:167], v[188:191], v[52:55]\n\t" \
    "v_mfma_f32_16x16x32_bf16 v[80:83], v[164:167], v[192:195], v[80:83]\n\t" \
    "v_mfma_f32_16x16x32_bf16 v[108:111], v[164:167], v[196:199], v[108:111]\n\t" \
    "global_load_dwordx4 v[164:167], v208, s[42:43] offset:3072\n\t" \
    "s_add_u32 s42, s42, 7168\n\t" \
    "s_addc_u32 s43, s43, 0\n\t" \
    "s_add_u32 s40, s40, 128\n\t" \
    "s_addc_u32 s41, s41, 0\n\t" \
    "s_waitcnt vmcnt(17)\n\t" \
    "s_waitcnt lgkmcnt(0)\n\t" \
    "v_mfma_f32_16x16x32_bf16 v[0:3], v[112:115], v[168:171], v[0:3]\n\t" \
    "v_mfma_f32_16x16x32_bf16 v[28:31], v[112:115], v[172:175], v[28:31]\n\t" \
    "ds_read_b128 v[184:187], v211 offset:0\n\t" \
    "ds_read_b128 v[188:191], v211 offset:2048\n\t" \
    "ds_read_b128 v[192:195], v211 offset:4096\n\t" \
    "ds_read_b128 v[196:199], v211 offset:6144\n\t" \
    "v_mfma_f32_16x16x32_bf16 v[56:59], v[112:115], v[176:179], v[56:59]\n\t" \
    "v_mfma_f32_16x16x32_bf16 v[84:87], v[112:115], v[180:183], v[84:87]\n\t" \
    "global_load_dwordx4 v[112:115], v208, s[42:43] offset:-3072\n\t" \
    "v_mfma_f32_16x16x32_bf16 v[4:7], v[116:119], v[168:171], v[4:7]\n\t" \
    "v_mfma_f32_16x16x32_bf16 v[32:35], v[116:119], v[172:175], v[32:35]\n\t" \
    "v_mfma_f32_16x16x32_bf16 v[60:63], v[116:119], v[176:179], v[60:63]\n\t" \
    "v_mfma_f32_16x16x32_bf16 v[88:91], v[116:119], v[180:183], v[88:91]\n\t" \
    "global_load_dwordx4 v[116:119], v208, s[42:43] offset:-2048\n\t" \
    "v_mfma_f32_16x16x32_bf16 v[8:11], v[120:123], v[168:171], v[8:11]\n\t" \
    "v_mfma_f32_16x16x32_bf16 v[36:39], v[120:123], v[172:175], v[36:39]\n\t" \
    "v_mfma_f32_16x16x32_bf16 v[64:67], v[120:123], v[176:179], v[64:67]\n\t" \
    "v_mfma_f32_16x16x32_bf16 v[92:95], v[120:123], v[180:183], v[92:95]\n\t" \
    "global_load_dwordx4 v[120:123], v208, s[42:43] offset:-1024\n\t" \
    "s_waitcnt vmcnt(14)\n\t" \
    "v_mfma_f32_16x16x32_bf16 v[12:15], v[124:127], v[168:171], v[12:15]\n\t" \
    "v_mfma_f32_16x16x32_bf16 v[40:43], v[124:127], v[172:175], v[40:43]\n\t" \
    "v_mfma_f32_16x16x32_bf16 v[68:71], v[124:127], v[176:179], v[68:71]\n\t" \
    "v_mfma_f32_16x16x32_bf16 v[96:99], v[124:127], v[180:183], v[96:99]\n\t" \
    "global_load_dwordx4 v[124:127], v208, s[42:43] offset:0\n\t" \
    "v_mfma_f32_16x16x32_bf16 v[16:19], v[128:131], v[168:171], v[16:19]\n\t" \
    "v_mfma_f32_16x16x32_bf16 v[44:47], v[128:131], v[172:175], v[44:47]\n\t" \
    "v_mfma_f32_16x16x32_bf16 v[72:75], v[128:131], v[176:179], v[72:75]\n\t" \
    "v_mfma_f32_16x16x32_bf16 v[100:103], v[128:131], v[180:183], v[100:103]\n\t" \
    "global_load_dwordx4 v[128:131], v208, s[42:43] offset:1024\n\t" \
    "v_mfma_f32_16x16x32_bf16 v[20:23], v[132:135], v[168:171], v[20:23]\n\t" \
    "v_mfma_f32_16x16x32_bf16 v[48:51], v[132:135], v[172:175], v[48:51]\n\t" \
    "v_mfma_f32_16x16x32_bf16 v[76:79], v[132:135], v[176:179], v[76:79]\n\t" \
    "v_mfma_f32_16x16x32_bf16 v[104:107], v[132:135], v[180:183], v[104:107]\n\t" \
    "global_load_dwordx4 v[132:135], v208, s[42:43] offset:2048\n\t" \
    "v_mfma_f32_16x16x32_bf16 v[24:27], v[136:139], v[168:171], v[24:27]\n\t" \
    "v_mfma_f32_16x16x32_bf16 v[52:55], v[136:139], v[172:175], v[52:55]\n\t" \
    "v_mfma_f32_16x16x32_bf16 v[80:83], v[136:139], v[176:179], v[80:83]\n\t" \
    "v_mfma_f32_16x16x32_bf16 v[108:111], v[136:139], v[180:183], v[108:111]\n\t" \
    "global_load_dwordx4 v[136:139], v208, s[42:43] offset:3072\n\t" \
    "s_add_u32 s42, s42, 7168\n\t" \
    "s_addc_u32 s43, s43, 0\n\t" \
    "s_waitcnt vmcnt(10)\n\t" \
    "s_waitcnt lgkmcnt(0)\n\t" \
    "v_mfma_f32_16x16x32_bf16 v[0:3], v[140:143], v[184:187], v[0:3]\n\t" \
    "v_mfma_f32_16x16x32_bf16 v[28:31], v[140:143], v[188:191], v[28:31]\n\t" \
    "ds_read_b128 v[168:171], v210 offset:8192\n\t" \
    "ds_read_b128 v[172:175], v210 offset:10240\n\t" \
    "ds_read_b128 v[176:179], v210 offset:12288\n\t" \
    "ds_read_b128 v[180:183], v210 offset:14336\n\t" \
    "v_mfma_f32_16x16x32_bf16 v[56:59], v[140:143], v[192:195], v[56:59]\n\t" \
    "v_mfma_f32_16x16x32_bf16 v[84:87], v[140:143], v[196:199], v[84:87]\n\t" \
    "global_load_dwordx4 v[140:143], v208, s[42:43] offset:-3072\n\t" \
    "v_mfma_f32_16x16x32_bf16 v[4:7], v[144:147], v[184:187], v[4:7]\n\t" \
    "v_mfma_f32_16x16x32_bf16 v[32:35], v[144:147], v[188:191], v[32:35]\n\t" \
    "v_mfma_f32_16x16x32_bf16 v[60:63], v[144:147], v[192:195], v[60:63]\n\t" \
    "v_mfma_f32_16x16x32_bf16 v[88:91], v[144:147], v[196:199], v[88:91]\n\t" \
    "global_load_dwordx4 v[144:147], v208, s[42:43] offset:-2048\n\t" \
    "v_mfma_f32_16x16x32_bf16 v[8:11], v[148:151], v[184:187], v[8:11]\n\t" \
    "v_mfma_f32_16x16x32_bf16 v[36:39], v[148:151], v[188:191], v[36:39]\n\t" \
    "v_mfma_f32_16x16x32_bf16 v[64:67], v[148:151], v[192:195], v[64:67]\n\t" \
    "v_mfma_f32_16x16x32_bf16 v[92:95], v[148:151], v[196:199], v[92:95]\n\t" \
    "global_load_dwordx4 v[148:151], v208, s[42:43] offset:-1024\n\t" \
    "s_waitcnt vmcnt(10)\n\t" \
    "v_mfma_f32_16x16x32_bf16 v[12:15], v[152:155], v[184:187], v[12:15]\n\t" \
    "v_mfma_f32_16x16x32_bf16 v[40:43], v[152:155], v[188:191], v[40:43]\n\t" \
    "v_mfma_f32_16x16x32_bf16 v[68:71], v[152:155], v[192:195], v[68:71]\n\t" \
    "v_mfma_f32_16x16x32_bf16 v[96:99], v[152:155], v[196:199], v[96:99]\n\t" \
    "global_load_dwordx4 v[152:155], v208, s[42:43] offset:0\n\t" \
    "v_mfma_f32_16x16x32_bf16 v[16:19], v[156:159], v[184:187], v[16:19]\n\t" \
    "v_mfma_f32_16x16x32_bf16 v[44:47], v[156:159], v[188:191], v[44:47]\n\t" \
    "v_mfma_f32_16x16x32_bf16 v[72:75], v[156:159], v[192:195], v[72:75]\n\t" \
    "v_mfma_f32_16x16x32_bf16 v[100:103], v[156:159], v[196:199], v[100:103]\n\t" \
    "global_load_dwordx4 v[156:159], v208, s[42:43] offset:1024\n\t" \
    "v_mfma_f32_16x16x32_bf16 v[20:23], v[160:163], v[184:187], v[20:23]\n\t" \
    "v_mfma_f32_16x16x32_bf16 v[48:51], v[160:163], v[188:191], v[48:51]\n\t" \
    "v_mfma_f32_16x16x32_bf16 v[76:79], v[160:163], v[192:195], v[76:79]\n\t" \
    "v_mfma_f32_16x16x32_bf16 v[104:107], v[160:163], v[196:199], v[104:107]\n\t" \
    "global_load_dwordx4 v[160:163], v208, s[42:43] offset:2048\n\t" \
    "v_mfma_f32_16x16x32_bf16 v[24:27], v[164:167], v[184:187], v[24:27]\n\t" \
    "v_mfma_f32_16x16x32_bf16 v[52:55], v[164:167], v[188:191], v[52:55]\n\t" \
    "v_mfma_f32_16x16x32_bf16 v[80:83], v[164:167], v[192:195], v[80:83]\n\t" \
    "v_mfma_f32_16x16x32_bf16 v[108:111], v[164:167], v[196:199], v[108:111]\n\t" \
    "global_load_dwordx4 v[164:167], v208, s[42:43] offset:3072\n\t" \
    "s_add_u32 s42, s42, 7168\n\t" \
    "s_addc_u32 s43, s43, 0\n\t" \
    "s_waitcnt vmcnt(11)\n\t" \
    "s_waitcnt lgkmcnt(0)\n\t" \
    "v_mfma_f32_16x16x32_bf16 v[0:3], v[112:115], v[168:171], v[0:3]\n\t" \
    "v_mfma_f32_16x16x32_bf16 v[28:31], v[112:115], v[172:175], v[28:31]\n\t" \
    "ds_read_b128 v[184:187], v211 offset:8192\n\t" \
    "ds_read_b128 v[188:191], v211 offset:10240\n\t" \
    "ds_read_b128 v[192:195], v211 offset:12288\n\t" \
    "ds_read_b128 v[196:199], v211 offset:14336\n\t" \
    "v_mfma_f32_16x16x32_bf16 v[56:59], v[112:115], v[176:179], v[56:59]\n\t" \
    "v_mfma_f32_16x16x32_bf16 v[84:87], v[112:115], v[180:183], v[84:87]\n\t" \
    "global_load_dwordx4 v[112:115], v208, s[48:49] offset:-4096\n\t" \
    "v_mfma_f32_16x16x32_bf16 v[4:7], v[116:119], v[168:171], v[4:7]\n\t" \
    "v_mfma_f32_16x16x32_bf16 v[32:35], v[116:119], v[172:175], v[32:35]\n\t" \
    "v_mfma_f32_16x16x32_bf16 v[60:63], v[116:119], v[176:179], v[60:63]\n\t" \
    "v_mfma_f32_16x16x32_bf16 v[88:91], v[116:119], v[180:183], v[88:91]\n\t" \
    "global_load_dwordx4 v[116:119], v208, s[50:51] offset:-4096\n\t" \
    "v_mfma_f32_16x16x32_bf16 v[8:11], v[120:123], v[168:171], v[8:11]\n\t" \
    "v_mfma_f32_16x16x32_bf16 v[36:39], v[120:123], v[172:175], v[36:39]\n\t" \
    "v_mfma_f32_16x16x32_bf16 v[64:67], v[120:123], v[176:179], v[64:67]\n\t" \
    "v_mfma_f32_16x16x32_bf16 v[92:95], v[120:123], v[180:183], v[92:95]\n\t" \
    "global_load_dwordx4 v[120:123], v208, s[48:49] offset:-3072\n\t" \
    "s_waitcnt vmcnt(10)\n\t" \
    "v_mfma_f32_16x16x32_bf16 v[12:15], v[124:127], v[168:171], v[12:15]\n\t" \
    "v_mfma_f32_16x16x32_bf16 v[40:43], v[124:127], v[172:175], v[40:43]\n\t" \
    "v_mfma_f32_16x16x32_bf16 v[68:71], v[124:127], v[176:179], v[68:71]\n\t" \
    "v_mfma_f32_16x16x32_bf16 v[96:99], v[124:127], v[180:183], v[96:99]\n\t" \
    "global_load_dwordx4 v[124:127], v208, s[50:51] offset:-3072\n\t" \
    "v_mfma_f32_16x16x32_bf16 v[16:19], v[128:131], v[168:171], v[16:19]\n\t" \
    "v_mfma_f32_16x16x32_bf16 v[44:47], v[128:131], v[172:175], v[44:47]\n\t" \
    "v_mfma_f32_16x16x32_bf16 v[72:75], v[128:131], v[176:179], v[72:75]\n\t" \
    "v_mfma_f32_16x16x32_bf16 v[100:103], v[128:131], v[180:183], v[100:103]\n\t" \
    "global_load_dwordx4 v[128:131], v208, s[48:49] offset:-2048\n\t" \
    "v_mfma_f32_16x16x32_bf16 v[20:23], v[132:135], v[168:171], v[20:23]\n\t" \
    "v_mfma_f32_16x16x32_bf16 v[48:51], v[132:135], v[172:175], v[48:51]\n\t" \
    "v_mfma_f32_16x16x32_bf16 v[76:79], v[132:135], v[176:179], v[76:79]\n\t" \
    "v_mfma_f32_16x16x32_bf16 v[104:107], v[132:135], v[180:183], v[104:107]\n\t" \
    "global_load_dwordx4 v[132:135], v208, s[50:51] offset:-2048\n\t" \
    "v_mfma_f32_16x16x32_bf16 v[24:27], v[136:139], v[168:171], v[24:27]\n\t" \
    "v_mfma_f32_16x16x32_bf16 v[52:55], v[136:139], v[172:175], v[52:55]\n\t" \
    "v_mfma_f32_16x16x32_bf16 v[80:83], v[136:139], v[176:179], v[80:83]\n\t" \
    "v_mfma_f32_16x16x32_bf16 v[108:111], v[136:139], v[180:183], v[108:111]\n\t" \
    "global_load_dwordx4 v[136:139], v208, s[48:49] offset:-1024\n\t" \
    "s_waitcnt vmcnt(7)\n\t" \
    "s_waitcnt lgkmcnt(0)\n\t" \
    "v_mfma_f32_16x16x32_bf16 v[84:87], v[140:143], v[196:199], v[84:87]\n\t" \
    "v_mfma_f32_16x16x32_bf16 v[88:91], v[144:147], v[196:199], v[88:91]\n\t" \
    "global_load_dwordx4 v[168:171], v208, s[50:51] offset:-1024\n\t" \
    "global_load_dwordx4 v[172:175], v208, s[48:49] offset:0\n\t" \
    "v_mfma_f32_16x16x32_bf16 v[92:95], v[148:151], v[196:199], v[92:95]\n\t" \
    "v_mfma_f32_16x16x32_bf16 v[96:99], v[152:155], v[196:199], v[96:99]\n\t" \
    "global_load_dwordx4 v[176:179], v208, s[50:51] offset:0\n\t" \
    "global_load_dwordx4 v[180:183], v208, s[48:49] offset:1024\n\t" \
    "v_mfma_f32_16x16x32_bf16 v[100:103], v[156:159], v[196:199], v[100:103]\n\t" \
    "v_mfma_f32_16x16x32_bf16 v[104:107], v[160:163], v[196:199], v[104:107]\n\t" \
    "v_mfma_f32_16x16x32_bf16 v[108:111], v[164:167], v[196:199], v[108:111]\n\t" \
    "v_mfma_f32_16x16x32_bf16 v[56:59], v[140:143], v[192:195], v[56:59]\n\t" \
    "ds_write_b128 v209, v[84:87] offset:14336\n\t" \
    "ds_write_b128 v209, v[88:91] offset:15360\n\t" \
    "v_mfma_f32_16x16x32_bf16 v[60:63], v[144:147], v[192:195], v[60:63]\n\t" \
    "v_mfma_f32_16x16x32_bf16 v[64:67], v[148:151], v[192:195], v[64:67]\n\t" \
    "ds_write_b128 v209, v[92:95] offset:16384\n\t" \
    "ds_write_b128 v209, v[96:99] offset:17408\n\t" \
    "v_mfma_f32_16x16x32_bf16 v[68:71], v[152:155], v[192:195], v[68:71]\n\t" \
    "v_mfma_f32_16x16x32_bf16 v[72:75], v[156:159], v[192:195], v[72:75]\n\t" \
    "s_waitcnt lgkmcnt(2)\n\t" \
    "global_load_dwordx4 v[84:87], v208, s[50:51] offset:1024\n\t" \
    "global_load_dwordx4 v[88:91], v208, s[48:49] offset:2048\n\t" \
    "ds_write_b128 v209, v[100:103] offset:18432\n\t" \
    "ds_write_b128 v209, v[104:107] offset:19456\n\t" \
    "v_mfma_f32_16x16x32_bf16 v[76:79], v[160:163], v[192:195], v[76:79]\n\t" \
    "v_mfma_f32_16x16x32_bf16 v[80:83], v[164:167], v[192:195], v[80:83]\n\t" \
    "s_waitcnt lgkmcnt(2)\n\t" \
    "global_load_dwordx4 v[92:95], v208, s[50:51] offset:2048\n\t" \
    "global_load_dwordx4 v[96:99], v208, s[48:49] offset:3072\n\t" \
    "ds_write_b128 v209, v[108:111] offset:20480\n\t" \
    "ds_write_b128 v209, v[56:59] offset:7168\n\t" \
    "v_mfma_f32_16x16x32_bf16 v[28:31], v[140:143], v[188:191], v[28:31]\n\t" \
    "v_mfma_f32_16x16x32_bf16 v[32:35], v[144:147], v[188:191], v[32:35]\n\t" \
    "s_waitcnt lgkmcnt(2)\n\t" \
    "global_load_dwordx4 v[100:103], v208, s[50:51] offset:3072\n\t" \
    "s_add_u32 s48, s48, 8192\n\t" \
    "s_addc_u32 s49, s49, 0\n\t" \
    "s_add_u32 s50, s50, 8192\n\t" \
    "s_addc_u32 s51, s51, 0\n\t" \
    "global_load_dwordx4 v[104:107], v208, s[48:49] offset:-4096\n\t" \
    "ds_write_b128 v209, v[60:63] offset:8192\n\t" \
    "ds_write_b128 v209, v[64:67] offset:9216\n\t" \
    "v_mfma_f32_16x16x32_bf16 v[36:39], v[148:151], v[188:191], v[36:39]\n\t" \
    "v_mfma_f32_16x16x32_bf16 v[40:43], v[152:155], v[188:191], v[40:43]\n\t" \
    "s_waitcnt lgkmcnt(2)\n\t" \
    "global_load_dwordx4 v[108:111], v208, s[50:51] offset:-4096\n\t" \
    "ds_write_b128 v209, v[68:71] offset:10240\n\t" \
    "ds_write_b128 v209, v[72:75] offset:11264\n\t" \
    "v_mfma_f32_16x16x32_bf16 v[44:47], v[156:159], v[188:191], v[44:47]\n\t" \
    "v_mfma_f32_16x16x32_bf16 v[48:51], v[160:163], v[188:191], v[48:51]\n\t" \
    "s_waitcnt lgkmcnt(2)\n\t" \
    "global_load_dwordx4 v[64:67], v208, s[48:49] offset:-3072\n\t" \
    "ds_write_b128 v209, v[76:79] offset:12288\n\t" \
    "ds_write_b128 v209, v[80:83] offset:13312\n\t" \
    "v_mfma_f32_16x16x32_bf16 v[52:55], v[164:167], v[188:191], v[52:55]\n\t" \
    "v_mfma_f32_16x16x32_bf16 v[0:3], v[140:143], v[184:187], v[0:3]\n\t" \
    "s_waitcnt lgkmcnt(2)\n\t" \
    "global_load_dwordx4 v[68:71], v208, s[50:51] offset:-3072\n\t" \
    "global_load_dwordx4 v[72:75], v208, s[48:49] offset:-2048\n\t" \
    "ds_write_b128 v209, v[28:31] offset:0\n\t" \
    "ds_write_b128 v209, v[32:35] offset:1024\n\t" \
    "v_mfma_f32_16x16x32_bf16 v[4:7], v[144:147], v[184:187], v[4:7]\n\t" \
    "v_mfma_f32_16x16x32_bf16 v[8:11], v[148:151], v[184:187], v[8:11]\n\t" \
    "s_waitcnt lgkmcnt(2)\n\t" \
    "global_load_dwordx4 v[76:79], v208, s[50:51] offset:-2048\n\t" \
    "global_load_dwordx4 v[80:83], v208, s[48:49] offset:-1024\n\t" \
    "ds_write_b128 v209, v[36:39] offset:2048\n\t" \
    "ds_write_b128 v209, v[40:43] offset:3072\n\t" \
    "v_mfma_f32_16x16x32_bf16 v[12:15], v[152:155], v[184:187], v[12:15]\n\t" \
    "v_mfma_f32_16x16x32_bf16 v[16:19], v[156:159], v[184:187], v[16:19]\n\t" \
    "global_load_dwordx4 v[140:143], v208, s[50:51] offset:-1024\n\t" \
    "global_load_dwordx4 v[144:147], v208, s[48:49] offset:0\n\t" \
    "ds_write_b128 v209, v[44:47] offset:4096\n\t" \
    "ds_write_b128 v209, v[48:51] offset:5120\n\t" \
    "v_mfma_f32_16x16x32_bf16 v[20:23], v[160:163], v[184:187], v[20:23]\n\t" \
    "v_mfma_f32_16x16x32_bf16 v[24:27], v[164:167], v[184:187], v[24:27]\n\t" \
    "global_load_dwordx4 v[148:151], v208, s[50:51] offset:0\n\t" \
    "global_load_dwordx4 v[152:155], v208, s[48:49] offset:1024\n\t" \
    "ds_write_b128 v209, v[52:55] offset:6144\n\t" \
    "global_load_dwordx4 v[156:159], v208, s[50:51] offset:1024\n\t" \
    "s_nop 7\n\t"
#define GEMM_CLOB "v28", "v29", "v30", "v31", "v32", "v33", "v34", "v35", "v36", "v37", "v38", "v39", "v40", "v41", "v42", "v43", "v44", "v45", "v46", "v47", "v48", "v49", "v50", "v51", "v52", "v53", "v54", "v55", "v56", "v57", "v58", "v59", "v60", "v61", "v62", "v63", "v160", "v161", "v162", "v163", "v164", "v165", "v166", "v167", "v184", "v185", "v186", "v187", "v188", "v189", "v190", "v191", "v192", "v193", "v194", "v195", "v196", "v197", "v198", "v199", "v242", "v243", "v244", "v245", "v246", "v247"
#define P3A_MAIN \
    "s_waitcnt vmcnt(0)\n\t" \
    "v_mov_b32 v16, 1\n\t" \
    "s_mov_b64 exec, 1\n\t" \
    "global_store_dword %[fo], v16, %[ws] sc1\n\t" \
    "s_mov_b64 exec, -1\n\t" \
    "s_cmp_lt_u32 %[act], 28\n\t" \
    "s_cbranch_scc1 5f\n\t" \
    "s_endpgm\n\t" \
    "5:\n\t" \
    "s_setprio 0\n\t" \
    "global_load_dwordx4 %[xr], %[xo], %[xb]\n\t" \
    "s_mov_b32 %[cnt], 0x400000\n\t" \
    "v_mov_b32 v17, %[lc]\n\t" \
    "s_cmp_lg_u32 %[wi], 0\n\t" \
    "s_cbranch_scc1 8f\n\t" \
    "s_branch 6f\n\t" \
    "4:\n\t" \
    "s_sleep 1\n\t" \
    "6:\n\t" \
    "global_load_dword v16, %[po], %[ws] sc1\n\t" \
    "s_sub_u32 %[cnt], %[cnt], 1\n\t" \
    "s_cselect_b64 s[48:49], -1, 0\n\t" \
    "s_max_i32 %[cnt], %[cnt], 0\n\t" \
    "s_waitcnt vmcnt(0)\n\t" \
    "v_cmp_eq_u32 vcc, 1, v16\n\t" \
    "s_nop 4\n\t" \
    "s_or_b64 s[48:49], s[48:49], vcc\n\t" \
    "v_mov_b32 v18, s48\n\t" \
    "v_mov_b32 v19, s49\n\t" \
    "s_mov_b64 exec, 1\n\t" \
    "ds_write_b64 v17, v[18:19]\n\t" \
    "s_mov_b64 exec, -1\n\t" \
    "s_andn2_b32 s50, 0xff, s48\n\t" \
    "s_cbranch_scc1 4b\n\t" \
    "s_branch 7f\n\t" \
    "8:\n\t" \
    "s_sleep 1\n\t" \
    "ds_read_b32 v16, v17 offset:0\n\t" \
    "s_waitcnt lgkmcnt(0)\n\t" \
    "v_readfirstlane_b32 s50, v16\n\t" \
    "s_mov_b64 s[48:49], -1\n\t" \
    "s_nop 0\n\t" \
    "s_andn2_b32 s50, %[msk], s50\n\t" \
    "s_cbranch_scc1 8b\n\t" \
    "7:\n\t" \
    "global_load_dwordx4 v[64:67], %[vo], s[44:45] offset:-4096 sc1\n\t" \
    "global_load_dwordx4 v[68:71], %[vo], s[46:47] offset:-4096 sc1\n\t" \
    "global_load_dwordx4 v[72:75], %[vo], s[44:45] offset:-3072 sc1\n\t" \
    "global_load_dwordx4 v[76:79], %[vo], s[46:47] offset:-3072 sc1\n\t" \
    "global_load_dwordx4 v[80:83], %[vo], s[44:45] offset:-2048 sc1\n\t" \
    "global_load_dwordx4 v[84:87], %[vo], s[46:47] offset:-2048 sc1\n\t" \
    "global_load_dwordx4 v[88:91], %[vo], s[44:45] offset:-1024 sc1\n\t" \
    "global_load_dwordx4 v[92:95], %[vo], s[46:47] offset:-1024 sc1\n\t" \
    "global_load_dwordx4 v[96:99], %[vo], s[44:45] offset:0 sc1\n\t" \
    "global_load_dwordx4 v[100:103], %[vo], s[46:47] offset:0 sc1\n\t" \
    "global_load_dwordx4 v[104:107], %[vo], s[44:45] offset:1024 sc1\n\t" \
    "global_load_dwordx4 v[108:111], %[vo], s[46:47] offset:1024 sc1\n\t" \
    "global_load_dwordx4 v[112:115], %[vo], s[44:45] offset:2048 sc1\n\t" \
    "global_load_dwordx4 v[116:119], %[vo], s[46:47] offset:2048 sc1\n\t" \
    "global_load_dwordx4 v[120:123], %[vo], s[44:45] offset:3072 sc1\n\t" \
    "global_load_dwordx4 v[124:127], %[vo], s[46:47] offset:3072 sc1\n\t" \
    "s_branch 3f\n\t" \
    "2:\n\t" \
    "global_load_dword v16, %[po], %[ws] sc1\n\t" \
    "s_sub_u32 %[cnt], %[cnt], 1\n\t" \
    "s_cselect_b64 s[48:49], -1, 0\n\t" \
    "s_max_i32 %[cnt], %[cnt], 0\n\t" \
    "s_waitcnt vmcnt(0)\n\t" \
    "v_cmp_eq_u32 vcc, 1, v16\n\t" \
    "s_nop 4\n\t" \
    "s_or_b64 s[48:49], s[48:49], vcc\n\t" \
    "v_mov_b32 v18, s48\n\t" \
    "v_mov_b32 v19, s49\n\t" \
    "s_mov_b64 exec, 1\n\t" \
    "ds_write_b64 v17, v[18:19]\n\t" \
    "s_mov_b64 exec, -1\n\t" \
    "s_sleep 1\n\t" \
    "3:\n\t" \
    "s_not_b64 s[50:51], s[48:49]\n\t" \
    "s_cbranch_scc1 2b\n\t" \
    "s_waitcnt vmcnt(15)\n\t" \
    "v_mfma_f32_32x32x16_bf16 v[0:15], v[192:195], v[64:67], 0\n\t" \
    "v_mfma_f32_32x32x16_bf16 v[16:31], v[196:199], v[64:67], 0\n\t" \
    "s_waitcnt vmcnt(14)\n\t" \
    "v_mfma_f32_32x32x16_bf16 v[32:47], v[192:195], v[68:71], 0\n\t" \
    "v_mfma_f32_32x32x16_bf16 v[48:63], v[196:199], v[68:71], 0\n\t" \
    "s_waitcnt vmcnt(13)\n\t" \
    "v_mfma_f32_32x32x16_bf16 v[0:15], v[200:203], v[72:75], v[0:15]\n\t" \
    "v_mfma_f32_32x32x16_bf16 v[16:31], v[204:207], v[72:75], v[16:31]\n\t" \
    "s_waitcnt vmcnt(12)\n\t" \
    "v_mfma_f32_32x32x16_bf16 v[32:47], v[200:203], v[76:79], v[32:47]\n\t" \
    "v_mfma_f32_32x32x16_bf16 v[48:63], v[204:207], v[76:79], v[48:63]\n\t" \
    "s_waitcnt vmcnt(11)\n\t" \
    "v_mfma_f32_32x32x16_bf16 v[0:15], v[208:211], v[80:83], v[0:15]\n\t" \
    "v_mfma_f32_32x32x16_bf16 v[16:31], v[212:215], v[80:83], v[16:31]\n\t" \
    "s_waitcnt vmcnt(10)\n\t" \
    "v_mfma_f32_32x32x16_bf16 v[32:47], v[208:211], v[84:87], v[32:47]\n\t" \
    "v_mfma_f32_32x32x16_bf16 v[48:63], v[212:215], v[84:87], v[48:63]\n\t" \
    "s_waitcnt vmcnt(9)\n\t" \
    "v_mfma_f32_32x32x16_bf16 v[0:15], v[216:219], v[88:91], v[0:15]\n\t" \
    "v_mfma_f32_32x32x16_bf16 v[16:31], v[220:223], v[88:91], v[16:31]\n\t" \
    "s_waitcnt vmcnt(8)\n\t" \
    "v_mfma_f32_32x32x16_bf16 v[32:47], v[216:219], v[92:95], v[32:47]\n\t" \
    "v_mfma_f32_32x32x16_bf16 v[48:63], v[220:223], v[92:95], v[48:63]\n\t" \
    "s_waitcnt vmcnt(7)\n\t" \
    "v_mfma_f32_32x32x16_bf16 v[0:15], v[224:227], v[96:99], v[0:15]\n\t" \
    "v_mfma_f32_32x32x16_bf16 v[16:31], v[228:231], v[96:99], v[16:31]\n\t" \
    "s_waitcnt vmcnt(6)\n\t" \
    "v_mfma_f32_32x32x16_bf16 v[32:47], v[224:227], v[100:103], v[32:47]\n\t" \
    "v_mfma_f32_32x32x16_bf16 v[48:63], v[228:231], v[100:103], v[48:63]\n\t" \
    "s_waitcnt vmcnt(5)\n\t" \
    "v_mfma_f32_32x32x16_bf16 v[0:15], v[232:235], v[104:107], v[0:15]\n\t" \
    "v_mfma_f32_32x32x16_bf16 v[16:31], v[236:239], v[104:107], v[16:31]\n\t" \
    "s_waitcnt vmcnt(4)\n\t" \
    "v_mfma_f32_32x32x16_bf16 v[32:47], v[232:235], v[108:111], v[32:47]\n\t" \
    "v_mfma_f32_32x32x16_bf16 v[48:63], v[236:239], v[108:111], v[48:63]\n\t" \
    "s_waitcnt vmcnt(3)\n\t" \
    "v_mfma_f32_32x32x16_bf16 v[0:15], v[240:243], v[112:115], v[0:15]\n\t" \
    "v_mfma_f32_32x32x16_bf16 v[16:31], v[244:247], v[112:115], v[16:31]\n\t" \
    "s_waitcnt vmcnt(2)\n\t" \
    "v_mfma_f32_32x32x16_bf16 v[32:47], v[240:243], v[116:119], v[32:47]\n\t" \
    "v_mfma_f32_32x32x16_bf16 v[48:63], v[244:247], v[116:119], v[48:63]\n\t" \
    "s_waitcnt vmcnt(1)\n\t" \
    "v_mfma_f32_32x32x16_bf16 v[0:15], v[248:251], v[120:123], v[0:15]\n\t" \
    "v_mfma_f32_32x32x16_bf16 v[16:31], v[252:255], v[120:123], v[16:31]\n\t" \
    "s_waitcnt vmcnt(0)\n\t" \
    "v_mfma_f32_32x32x16_bf16 v[32:47], v[248:251], v[124:127], v[32:47]\n\t" \
    "v_mfma_f32_32x32x16_bf16 v[48:63], v[252:255], v[124:127], v[48:63]\n\t" \
    "s_nop 8\n\t" \
    "ds_write_b128 %[w0], v[8:11]\n\t" \
    "ds_write_b128 %[w0], v[12:15] offset:1024\n\t" \
    "ds_write_b128 %[w1], v[16:19] offset:0\n\t" \
    "ds_write_b128 %[w1], v[20:23] offset:1024\n\t" \
    "ds_write_b128 %[w1], v[24:27] offset:2048\n\t" \
    "ds_write_b128 %[w1], v[28:31] offset:3072\n\t" \
    "ds_write_b128 %[w2], v[32:35] offset:0\n\t" \
    "ds_write_b128 %[w2], v[36:39] offset:1024\n\t" \
    "ds_write_b128 %[w2], v[40:43] offset:2048\n\t" \
    "ds_write_b128 %[w2], v[44:47] offset:3072\n\t" \
    "s_nop 0\n\t" \
    "ds_write_b128 %[w3], v[48:51] offset:0\n\t" \
    "ds_write_b128 %[w3], v[52:55] offset:1024\n\t" \
    "ds_write_b128 %[w3], v[56:59] offset:2048\n\t" \
    "ds_write_b128 %[w3], v[60:63] offset:3072\n\t" \
    "s_waitcnt lgkmcnt(0)\n\t" \
    "s_barrier\n\t"
#define P3A_CLOB "s48", "s49", "s50", "s51", "v8", "v9", "v10", "v11", "v12", "v13", "v14", "v15", "v16", "v17", "v18", "v19", "v20", "v21", "v22", "v23", "v24", "v25", "v26", "v27", "v28", "v29", "v30", "v31", "v32", "v33", "v34", "v35", "v36", "v37", "v38", "v39", "v40", "v41", "v42", "v43", "v44", "v45", "v46", "v47", "v48", "v49", "v50", "v51", "v52", "v53", "v54", "v55", "v56", "v57", "v58", "v59", "v60", "v61", "v62", "v63", "v64", "v65", "v66", "v67", "v68", "v69", "v70", "v71", "v72", "v73", "v74", "v75", "v76", "v77", "v78", "v79", "v80", "v81", "v82", "v83", "v84", "v85", "v86", "v87", "v88", "v89", "v90", "v91", "v92", "v93", "v94", "v95", "v96", "v97", "v98", "v99", "v100", "v101", "v102", "v103", "v104", "v105", "v106", "v107", "v108", "v109", "v110", "v111", "v112", "v113", "v114", "v115", "v116", "v117", "v118", "v119", "v120", "v121", "v122", "v123", "v124", "v125", "v126", "v127"
#define P3G_MAIN \
    "s_waitcnt vmcnt(0)\n\t" \
    "v_mov_b32 v16, 1\n\t" \
    "s_mov_b64 exec, 1\n\t" \
    "global_store_dword %[fo], v16, %[ws] sc1\n\t" \
    "s_mov_b64 exec, -1\n\t" \
    "s_cmp_lt_u32 %[act], 28\n\t" \
    "s_cbranch_scc1 5f\n\t" \
    "s_endpgm\n\t" \
    "5:\n\t" \
    "s_setprio 0\n\t" \
    "global_load_dwordx4 %[xr], %[xo], %[xb]\n\t" \
    "s_mov_b32 %[cnt], 0x400000\n\t" \
    "v_mov_b32 v17, %[lc]\n\t" \
    "8:\n\t" \
    "s_sleep 1\n\t" \
    "ds_read_b32 v16, v17 offset:4\n\t" \
    "s_waitcnt lgkmcnt(0)\n\t" \
    "v_readfirstlane_b32 s50, v16\n\t" \
    "s_mov_b64 s[48:49], -1\n\t" \
    "s_nop 0\n\t" \
    "s_andn2_b32 s50, %[msk], s50\n\t" \
    "s_cbranch_scc1 8b\n\t" \
    "7:\n\t" \
    "global_load_dwordx4 v[160:163], %[vo], s[44:45] offset:-4096 sc1\n\t" \
    "global_load_dwordx4 v[164:167], %[vo], s[46:47] offset:-4096 sc1\n\t" \
    "global_load_dwordx4 v[184:187], %[vo], s[44:45] offset:-3072 sc1\n\t" \
    "global_load_dwordx4 v[188:191], %[vo], s[46:47] offset:-3072 sc1\n\t" \
    "global_load_dwordx4 v[192:195], %[vo], s[44:45] offset:-2048 sc1\n\t" \
    "global_load_dwordx4 v[196:199], %[vo], s[46:47] offset:-2048 sc1\n\t" \
    "global_load_dwordx4 v[200:203], %[vo], s[44:45] offset:-1024 sc1\n\t" \
    "global_load_dwordx4 v[204:207], %[vo], s[46:47] offset:-1024 sc1\n\t" \
    "global_load_dwordx4 v[208:211], %[vo], s[44:45] offset:0 sc1\n\t" \
    "global_load_dwordx4 v[212:215], %[vo], s[46:47] offset:0 sc1\n\t" \
    "global_load_dwordx4 v[216:219], %[vo], s[44:45] offset:1024 sc1\n\t" \
    "global_load_dwordx4 v[220:223], %[vo], s[46:47] offset:1024 sc1\n\t" \
    "global_load_dwordx4 v[224:227], %[vo], s[44:45] offset:2048 sc1\n\t" \
    "global_load_dwordx4 v[228:231], %[vo], s[46:47] offset:2048 sc1\n\t" \
    "global_load_dwordx4 v[232:235], %[vo], s[44:45] offset:3072 sc1\n\t" \
    "global_load_dwordx4 v[236:239], %[vo], s[46:47] offset:3072 sc1\n\t" \
    "s_waitcnt vmcnt(15)\n\t" \
    "v_mfma_f32_32x32x16_bf16 v[0:15], v[112:115], v[160:163], 0\n\t" \
    "v_mfma_f32_32x32x16_bf16 v[16:31], v[116:119], v[160:163], 0\n\t" \
    "s_waitcnt vmcnt(14)\n\t" \
    "v_mfma_f32_32x32x16_bf16 v[32:47], v[112:115], v[164:167], 0\n\t" \
    "v_mfma_f32_32x32x16_bf16 v[48:63], v[116:119], v[164:167], 0\n\t" \
    "s_add_u32 s44, s44, 8192\n\t" \
    "s_addc_u32 s45, s45, 0\n\t" \
    "s_add_u32 s46, s46, 8192\n\t" \
    "s_addc_u32 s47, s47, 0\n\t" \
    "global_load_dwordx4 v[160:163], %[vo], s[44:45] offset:-4096 sc1\n\t" \
    "global_load_dwordx4 v[164:167], %[vo], s[46:47] offset:-4096 sc1\n\t" \
    "s_waitcnt vmcnt(15)\n\t" \
    "v_mfma_f32_32x32x16_bf16 v[0:15], v[120:123], v[184:187], v[0:15]\n\t" \
    "v_mfma_f32_32x32x16_bf16 v[16:31], v[124:127], v[184:187], v[16:31]\n\t" \
    "s_waitcnt vmcnt(14)\n\t" \
    "v_mfma_f32_32x32x16_bf16 v[32:47], v[120:123], v[188:191], v[32:47]\n\t" \
    "v_mfma_f32_32x32x16_bf16 v[48:63], v[124:127], v[188:191], v[48:63]\n\t" \
    "global_load_dwordx4 v[184:187], %[vo], s[44:45] offset:-3072 sc1\n\t" \
    "global_load_dwordx4 v[188:191], %[vo], s[46:47] offset:-3072 sc1\n\t" \
    "s_waitcnt vmcnt(15)\n\t" \
    "v_mfma_f32_32x32x16_bf16 v[0:15], v[128:131], v[192:195], v[0:15]\n\t" \
    "v_mfma_f32_32x32x16_bf16 v[16:31], v[132:135], v[192:195], v[16:31]\n\t" \
    "s_waitcnt vmcnt(14)\n\t" \
    "v_mfma_f32_32x32x16_bf16 v[32:47], v[128:131], v[196:199], v[32:47]\n\t" \
    "v_mfma_f32_32x32x16_bf16 v[48:63], v[132:135], v[196:199], v[48:63]\n\t" \
    "global_load_dwordx4 v[192:195], %[vo], s[44:45] offset:-2048 sc1\n\t" \
    "global_load_dwordx4 v[196:199], %[vo], s[46:47] offset:-2048 sc1\n\t" \
    "s_waitcnt vmcnt(15)\n\t" \
    "v_mfma_f32_32x32x16_bf16 v[0:15], v[136:139], v[200:203], v[0:15]\n\t" \
    "v_mfma_f32_32x32x16_bf16 v[16:31], v[168:171], v[200:203], v[16:31]\n\t" \
    "s_waitcnt vmcnt(14)\n\t" \
    "v_mfma_f32_32x32x16_bf16 v[32:47], v[136:139], v[204:207], v[32:47]\n\t" \
    "v_mfma_f32_32x32x16_bf16 v[48:63], v[168:171], v[204:207], v[48:63]\n\t" \
    "global_load_dwordx4 v[200:203], %[vo], s[44:45] offset:-1024 sc1\n\t" \
    "global_load_dwordx4 v[204:207], %[vo], s[46:47] offset:-1024 sc1\n\t" \
    "s_waitcnt vmcnt(15)\n\t" \
    "v_mfma_f32_32x32x16_bf16 v[0:15], v[172:175], v[208:211], v[0:15]\n\t" \
    "v_mfma_f32_32x32x16_bf16 v[16:31], v[176:179], v[208:211], v[16:31]\n\t" \
    "s_waitcnt vmcnt(14)\n\t" \
    "v_mfma_f32_32x32x16_bf16 v[32:47], v[172:175], v[212:215], v[32:47]\n\t" \
    "v_mfma_f32_32x32x16_bf16 v[48:63], v[176:179], v[212:215], v[48:63]\n\t" \
    "global_load_dwordx4 v[208:211], %[vo], s[44:45] offset:0 sc1\n\t" \
    "global_load_dwordx4 v[212:215], %[vo], s[46:47] offset:0 sc1\n\t" \
    "s_waitcnt vmcnt(15)\n\t" \
    "v_mfma_f32_32x32x16_bf16 v[0:15], v[180:183], v[216:219], v[0:15]\n\t" \
    "v_mfma_f32_32x32x16_bf16 v[16:31], v[84:87], v[216:219], v[16:31]\n\t" \
    "s_waitcnt vmcnt(14)\n\t" \
    "v_mfma_f32_32x32x16_bf16 v[32:47], v[180:183], v[220:223], v[32:47]\n\t" \
    "v_mfma_f32_32x32x16_bf16 v[48:63], v[84:87], v[220:223], v[48:63]\n\t" \
    "global_load_dwordx4 v[216:219], %[vo], s[44:45] offset:1024 sc1\n\t" \
    "global_load_dwordx4 v[220:223], %[vo], s[46:47] offset:1024 sc1\n\t" \
    "s_waitcnt vmcnt(15)\n\t" \
    "v_mfma_f32_32x32x16_bf16 v[0:15], v[88:91], v[224:227], v[0:15]\n\t" \
    "v_mfma_f32_32x32x16_bf16 v[16:31], v[92:95], v[224:227], v[16:31]\n\t" \
    "s_waitcnt vmcnt(14)\n\t" \
    "v_mfma_f32_32x32x16_bf16 v[32:47], v[88:91], v[228:231], v[32:47]\n\t" \
    "v_mfma_f32_32x32x16_bf16 v[48:63], v[92:95], v[228:231], v[48:63]\n\t" \
    "s_waitcnt vmcnt(13)\n\t" \
    "v_mfma_f32_32x32x16_bf16 v[0:15], v[96:99], v[232:235], v[0:15]\n\t" \
    "v_mfma_f32_32x32x16_bf16 v[16:31], v[100:103], v[232:235], v[16:31]\n\t" \
    "s_waitcnt vmcnt(12)\n\t" \
    "v_mfma_f32_32x32x16_bf16 v[32:47], v[96:99], v[236:239], v[32:47]\n\t" \
    "v_mfma_f32_32x32x16_bf16 v[48:63], v[100:103], v[236:239], v[48:63]\n\t" \
    "s_waitcnt vmcnt(11)\n\t" \
    "v_mfma_f32_32x32x16_bf16 v[0:15], v[104:107], v[160:163], v[0:15]\n\t" \
    "v_mfma_f32_32x32x16_bf16 v[16:31], v[108:111], v[160:163], v[16:31]\n\t" \
    "s_waitcnt vmcnt(10)\n\t" \
    "v_mfma_f32_32x32x16_bf16 v[32:47], v[104:107], v[164:167], v[32:47]\n\t" \
    "v_mfma_f32_32x32x16_bf16 v[48:63], v[108:111], v[164:167], v[48:63]\n\t" \
    "s_waitcnt vmcnt(9)\n\t" \
    "v_mfma_f32_32x32x16_bf16 v[0:15], v[64:67], v[184:187], v[0:15]\n\t" \
    "v_mfma_f32_32x32x16_bf16 v[16:31], v[68:71], v[184:187], v[16:31]\n\t" \
    "s_waitcnt vmcnt(8)\n\t" \
    "v_mfma_f32_32x32x16_bf16 v[32:47], v[64:67], v[188:191], v[32:47]\n\t" \
    "v_mfma_f32_32x32x16_bf16 v[48:63], v[68:71], v[188:191], v[48:63]\n\t" \
    "s_waitcnt vmcnt(7)\n\t" \
    "v_mfma_f32_32x32x16_bf16 v[0:15], v[72:75], v[192:195], v[0:15]\n\t" \
    "v_mfma_f32_32x32x16_bf16 v[16:31], v[76:79], v[192:195], v[16:31]\n\t" \
    "s_waitcnt vmcnt(6)\n\t" \
    "v_mfma_f32_32x32x16_bf16 v[32:47], v[72:75], v[196:199], v[32:47]\n\t" \
    "v_mfma_f32_32x32x16_bf16 v[48:63], v[76:79], v[196:199], v[48:63]\n\t" \
    "s_waitcnt vmcnt(5)\n\t" \
    "v_mfma_f32_32x32x16_bf16 v[0:15], v[80:83], v[200:203], v[0:15]\n\t" \
    "v_mfma_f32_32x32x16_bf16 v[16:31], v[140:143], v[200:203], v[16:31]\n\t" \
    "s_waitcnt vmcnt(4)\n\t" \
    "v_mfma_f32_32x32x16_bf16 v[32:47], v[80:83], v[204:207], v[32:47]\n\t" \
    "v_mfma_f32_32x32x16_bf16 v[48:63], v[140:143], v[204:207], v[48:63]\n\t" \
    "s_waitcnt vmcnt(3)\n\t" \
    "v_mfma_f32_32x32x16_bf16 v[0:15], v[144:147], v[208:211], v[0:15]\n\t" \
    "v_mfma_f32_32x32x16_bf16 v[16:31], v[148:151], v[208:211], v[16:31]\n\t" \
    "s_waitcnt vmcnt(2)\n\t" \
    "v_mfma_f32_32x32x16_bf16 v[32:47], v[144:147], v[212:215], v[32:47]\n\t" \
    "v_mfma_f32_32x32x16_bf16 v[48:63], v[148:151], v[212:215], v[48:63]\n\t" \
    "s_waitcnt vmcnt(1)\n\t" \
    "v_mfma_f32_32x32x16_bf16 v[0:15], v[152:155], v[216:219], v[0:15]\n\t" \
    "v_mfma_f32_32x32x16_bf16 v[16:31], v[156:159], v[216:219], v[16:31]\n\t" \
    "s_waitcnt vmcnt(0)\n\t" \
    "v_mfma_f32_32x32x16_bf16 v[32:47], v[152:155], v[220:223], v[32:47]\n\t" \
    "v_mfma_f32_32x32x16_bf16 v[48:63], v[156:159], v[220:223], v[48:63]\n\t" \
    "s_nop 8\n\t" \
    "ds_write_b128 %[w0], v[0:3]\n\t" \
    "ds_write_b128 %[w0], v[4:7] offset:1024\n\t" \
    "ds_write_b128 %[w1], v[16:19] offset:0\n\t" \
    "ds_write_b128 %[w1], v[20:23] offset:1024\n\t" \
    "ds_write_b128 %[w1], v[24:27] offset:2048\n\t" \
    "ds_write_b128 %[w1], v[28:31] offset:3072\n\t" \
    "ds_write_b128 %[w2], v[32:35] offset:0\n\t" \
    "ds_write_b128 %[w2], v[36:39] offset:1024\n\t" \
    "ds_write_b128 %[w2], v[40:43] offset:2048\n\t" \
    "ds_write_b128 %[w2], v[44:47] offset:3072\n\t" \
    "s_nop 0\n\t" \
    "ds_write_b128 %[w3], v[48:51] offset:0\n\t" \
    "ds_write_b128 %[w3], v[52:55] offset:1024\n\t" \
    "ds_write_b128 %[w3], v[56:59] offset:2048\n\t" \
    "ds_write_b128 %[w3], v[60:63] offset:3072\n\t" \
    "s_waitcnt lgkmcnt(0)\n\t" \
    "s_barrier\n\t"
#define P3G_CLOB "s48", "s49", "s50", "s51", "v0", "v1", "v2", "v3", "v4", "v5", "v6", "v7", "v16", "v17", "v18", "v19", "v20", "v21", "v22", "v23", "v24", "v25", "v26", "v27", "v28", "v29", "v30", "v31", "v32", "v33", "v34", "v35", "v36", "v37", "v38", "v39", "v40", "v41", "v42", "v43", "v44", "v45", "v46", "v47", "v48", "v49", "v50", "v51", "v52", "v53", "v54", "v55", "v56", "v57", "v58", "v59", "v60", "v61", "v62", "v63", "v160", "v161", "v162", "v163", "v164", "v165", "v166", "v167", "v184", "v185", "v186", "v187", "v188", "v189", "v190", "v191", "v192", "v193", "v194", "v195", "v196", "v197", "v198", "v199", "v200", "v201", "v202", "v203", "v204", "v205", "v206", "v207", "v208", "v209", "v210", "v211", "v212", "v213", "v214", "v215", "v216", "v217", "v218", "v219", "v220", "v221", "v222", "v223", "v224", "v225", "v226", "v227", "v228", "v229", "v230", "v231", "v232", "v233", "v234", "v235", "v236", "v237", "v238", "v239"

DI void group_arrive(LAS unsigned* cnt, int lane) {
    asm volatile("" ::: "memory");
    if (lane == 0) __hip_atomic_fetch_add(cnt, 1u, __ATOMIC_RELAXED, __HIP_MEMORY_SCOPE_WORKGROUP);
    asm volatile("" ::: "memory");
}
DI void group_wait(volatile LAS unsigned* cnt) {
    while (__builtin_amdgcn_readfirstlane(*cnt) < 4u) __builtin_amdgcn_s_sleep(1);
    asm volatile("" ::: "memory");
}

template <int ROLE>
DI void finish_out(const char* smem, int wi, unsigned lo, f32x8 mine, const u32x4 xr, char* dst) {
    f32x4 pv[14];
#pragma unroll
    for (int c = 0; c < 3; ++c)
#pragma unroll
        for (int g = 0; g < 2; ++g) {
            pv[4 * c + g] = *(const f32x4*)(smem + K2_RING + wi * 24576 + c * 7168 + ROLE * 2048 + g * 1024 + lo);
            pv[4 * c + 2 + g] = *(const f32x4*)(smem + K2_ACMB + wi * 12288 + c * 4096 + ROLE * 2048 + g * 1024 + lo);
        }
    const unsigned pa = ROLE == 0 ? K2_RING + (wi ^ 1) * 24576 + 4096 : K2_AML + wi * 2048;
    pv[12] = *(const f32x4*)(smem + pa + lo); pv[13] = *(const f32x4*)(smem + pa + 1024 + lo);
    SBAR();
#pragma unroll
    for (int i = 0; i < 14; ++i)
#pragma unroll
        for (int e = 0; e < 4; ++e) mine[4 * (i & 1) + e] += pv[i][e];
    float v[8];
#pragma unroll
    for (int e = 0; e < 4; ++e) {
        auto sw = __builtin_amdgcn_permlane32_swap(__float_as_uint(mine[e]), __float_as_uint(mine[4 + e]), false, false);
        v[e] = __uint_as_float(sw[0]); v[4 + e] = __uint_as_float(sw[1]);
    }
    u32x4 o;
#pragma unroll
    for (int e = 0; e < 4; ++e) o[e] = pk2(v[2 * e] + bf_lo(xr[e]), v[2 * e + 1] + bf_hi(xr[e]));
    *(u32x4*)dst = o;
}

__global__ __launch_bounds__(512, 2) void k2_mix(const u16* __restrict__ wsp, const u16* __restrict__ x, char* __restrict__ ws, u16* __restrict__ out) {
    extern __shared__ __attribute__((aligned(16))) char smem[];
    const int tid = threadIdx.x;
    const int w = __builtin_amdgcn_readfirstlane(tid >> 6);
    const int xcd = blockIdx.x & 7, idx = blockIdx.x >> 3;

    const unsigned i7 = ((unsigned)idx * 37u) >> 8;
    const int ort = xcd * 4 + (i7 & 3), oct = idx - 7 * i7, wi = w & 3;
    const unsigned lds0 = (unsigned)(uintptr_t)smem;
    if (w < 4) {
        __builtin_amdgcn_s_setprio(3);
        const int lane = lane_id(), r = lane & 31, h = lane >> 5;
        const int head = 2 * xcd + (idx >> 4), qb = idx & 15;
        const char* qp = ws + WS_QF + (size_t)(head * 64 + qb * 4) * 2048;
        const char* kvb = ws + WS_KV + (size_t)(head * 64 + w * 16) * 4096;
        const unsigned lo = lane * 16;
        if (lane < 4) ((unsigned*)(smem + K2_CNT))[lane] = 0u;
        const char* b0 = ws + WS_WC + (size_t)((oct * 2 + (wi & 1)) * 88 + 8 * wi + 4) * 1024;
        const char* b1 = ws + WS_WC + (size_t)((oct * 2 + (wi & 1 ^ 1)) * 88 + 8 * wi + 4) * 1024;
        f32x32 wb0, wb1;
        f32x16 o[4]; u32x16 qa, qc; f32x4 nm;
        asm volatile("" : "=v"(qa), "=v"(qc), "=v"(nm));
#pragma unroll 1
        for (int attempt = 0;; ++attempt) {

            {
                unsigned vo = lo + 4096; int cnt;
                asm volatile(ATT_ASM
                    : "={v[0:15]}"(o[0]), "={v[16:31]}"(o[1]), "={v[32:47]}"(o[2]), "={v[48:63]}"(o[3]),
                      "+{v[64:79]}"(qa), "+{v[80:95]}"(qc), "+{v[96:99]}"(nm), [vo] "+{v132}"(vo), [cnt] "=&s"(cnt),
                      "={v[192:223]}"(wb0), "={v[224:255]}"(wb1)
                    : "{s[48:49]}"(b0), "{s[50:51]}"(b1), [lo] "{v135}"(lo), [kv] "s"(kvb), [qb] "s"(qp), [qo] "{v133}"(lo + w * 2048), [slow] "s"(attempt),
                      [la] "{v134}"((unsigned)(uintptr_t)smem + K2_ACMB + w * 12288 + lo)
                    : "memory", "scc", ATT_CLOB);
            }

            float mag[4];
#pragma unroll
            for (int i = 0; i < 4; ++i) {
                const float il = __builtin_amdgcn_rcpf(o[i][12]);
                *(f32x2*)(smem + K2_AML + (((w ^ (3 - i)) * 4 + w) * 64 + lane) * 8) = f32x2{__builtin_amdgcn_logf(o[i][12]) - nm[i], il};
                float t = fabsf(o[i][13]) + fabsf(o[i][14]) + fabsf(o[i][15]);
                t = h ? il * 1.0e6f : t;
#pragma unroll
                for (int e = 0; e < 13; ++e) t += fabsf(o[i][e]);
                mag[i] = t;
            }
            if (__builtin_expect(!__any(!((mag[0] + mag[1]) + (mag[2] + mag[3]) < 1.0e36f)) || attempt, 1)) break;
            asm volatile("s_waitcnt vmcnt(0)" : "+{v[192:223]}"(wb0), "+{v[224:255]}"(wb1) :: "memory");
            u32x4 q[4][2]; float mref[4]; f32x16 zero;
#pragma unroll
            for (int i = 0; i < 16; ++i) zero[i] = 0.f;
#pragma unroll
            for (int i = 0; i < 4; ++i) {
#pragma unroll
                for (int e = 0; e < 8; ++e) q[i][e >> 2][e & 3] = i < 2 ? qa[8 * i + e] : qc[8 * (i - 2) + e];
                if (h) { q[i][1][2] = 0u; q[i][1][3] = 0u; }
                mref[i] = -3.0e38f;
            }
            unsigned ko = lo; PINV(ko);
#pragma unroll 1
            for (int kt = 0; kt < 16; ++kt) {
                const bf16x8 k0 = *(const bf16x8*)(kvb + kt * 4096 + ko), k1 = *(const bf16x8*)(kvb + kt * 4096 + 1024 + ko);
#pragma unroll
                for (int i = 0; i < 4; ++i) {
                    f32x16 s = MFMA32(k0, __builtin_bit_cast(bf16x8, q[i][0]), zero); s = MFMA32(k1, __builtin_bit_cast(bf16x8, q[i][1]), s);
                    float m = mref[i];
#pragma unroll
                    for (int e = 0; e < 16; e += 2) m = fmaxf(fmaxf(m, s[e]), s[e + 1]);
                    mref[i] = m;
                }
            }
#pragma unroll
            for (int i = 0; i < 4; ++i) {
                auto sw = __builtin_amdgcn_permlane32_swap(__float_as_uint(mref[i]), __float_as_uint(mref[i]), false, false);
                const float m = fmaxf(__uint_as_float(sw[0]), __uint_as_float(sw[1]));
                const unsigned a = pk2(-m, 0.f) & 0xffffu; const float r1 = -m - bf_lo(a);
                const unsigned b = pk2(r1, 0.f) & 0xffffu; const float r2 = r1 - bf_lo(b);
                const unsigned c = pk2(r2, 0.f) & 0xffffu;
                nm[i] = bf_lo(a) + bf_lo(b) + bf_lo(c);
                const unsigned u2 = h ? (a | (b << 16)) : q[i][1][2], u3 = h ? c : q[i][1][3];
                if (i < 2) { qa[8 * i + 6] = u2; qa[8 * i + 7] = u3; } else { qc[8 * (i - 2) + 6] = u2; qc[8 * (i - 2) + 7] = u3; }
            }
        }

        group_arrive((LAS unsigned*)(smem + K2_CNT), lane);
        group_wait((volatile LAS unsigned*)(smem + K2_CNT));

        {
            f32x2 ml[4]; f32x4 pv[12];
#pragma unroll
            for (int i = 0; i < 4; ++i) ml[i] = *(const f32x2*)(smem + K2_AML + ((w * 4 + (w ^ (3 - i))) * 64 + 32 + r) * 8);
#pragma unroll
            for (int i = 0; i < 3; ++i)
#pragma unroll
                for (int g = 0; g < 4; ++g) pv[i * 4 + g] = *(const f32x4*)(smem + K2_ACMB + (w ^ (3 - i)) * 12288 + (i * 4 + g) * 1024 + lo);
            SBAR();
            const float E = fmaxf(fmaxf(ml[0][0], ml[1][0]), fmaxf(ml[2][0], ml[3][0]));
            float f[4];
#pragma unroll
            for (int i = 0; i < 4; ++i) f[i] = __builtin_amdgcn_exp2f(ml[i][0] - E);
            const float inv = __builtin_amdgcn_rcpf((f[0] + f[1]) + (f[2] + f[3]));
#pragma unroll
            for (int i = 0; i < 4; ++i) f[i] = f[i] * inv * ml[i][1];
            const char* dst = ws + WS_CAT + (size_t)((qb * 4 + w) * 88 + head * 2) * 1024;
            unsigned pk[8];
#pragma unroll
            for (int g = 0; g < 4; ++g) {
                float v[4];
#pragma unroll
                for (int e = 0; e < 4; ++e) v[e] = fmaf(pv[8 + g][e], f[2], fmaf(pv[4 + g][e], f[1], fmaf(pv[g][e], f[0], o[3][4 * g + e] * f[3])));
                pk[2 * g] = pk2(v[0], v[1]); pk[2 * g + 1] = pk2(v[2], v[3]);
            }
            if (h) { pk[6] = 0u; pk[7] = 0u; }
            asm volatile("global_store_dwordx4 %0, %1, %3 sc1\n\tglobal_store_dwordx4 %0, %2, %3 offset:1024 sc1\n\ts_nop 1"
                         :: "v"(lo), "v"(u32x4{pk[0], pk[1], pk[2], pk[3]}), "v"(u32x4{pk[4], pk[5], pk[6], pk[7]}), "s"(dst) : "memory");
        }
        {
            const char* a0 = ws + WS_CAT + (size_t)((ort * 2 + (wi >> 1)) * 88 + 8 * wi + 4) * 1024;
            const char* a1 = ws + WS_CAT + (size_t)((ort * 2 + (wi >> 1 ^ 1)) * 88 + 8 * wi + 4) * 1024;
            f32x8 mine; u32x4 xr;
            const unsigned xo = (ort * 64 + (wi >> 1) * 32 + r) * 896 + (oct * 64 + (wi & 1) * 32 + 8 * h) * 2;
            int cnt;
            asm volatile(P3A_MAIN
                : "={v[0:7]}"(mine), [xr] "=&v"(xr), "+{s[44:45]}"(a0), "+{s[46:47]}"(a1), [cnt] "=&s"(cnt)
                : "{v[192:223]}"(wb0), "{v[224:255]}"(wb1), [vo] "{v136}"(lo), [xo] "{v137}"(xo), [xb] "s"(x), [ws] "s"(ws),
                  [fo] "{v138}"((unsigned)WS_FL + ((qb * 2 + (w >> 1)) * 64 + head * 2 + (w & 1)) * FLS), [po] "{v139}"((unsigned)WS_FL + (ort * 64 + lane) * FLS),
                  [act] "s"(idx), [wi] "s"(wi), [msk] "s"(0xffu << (8 * wi)), [lc] "s"(lds0 + K2_CNT + 8), [w0] "{v140}"(lds0 + K2_AML + wi * 2048 + lo),
                  [w1] "{v141}"(lds0 + K2_ACMB + (wi ^ 1) * 12288 + 8192 + lo), [w2] "{v142}"(lds0 + K2_ACMB + (wi ^ 2) * 12288 + 4096 + lo),
                  [w3] "{v143}"(lds0 + K2_ACMB + (wi ^ 3) * 12288 + lo)
                : "memory", "scc", "vcc", P3A_CLOB);
            finish_out<0>(smem, wi, lo, mine, xr, (char*)out + xo);
            asm volatile(".Lcode_tail:");
        }
    } else {
        __builtin_amdgcn_s_setprio(2);
        const int gw = w - 4;
        const int tt = 8 * (xcd >> 1) + (idx >> 2), ft = 4 * (xcd & 1) + (idx & 3);
        const unsigned ring = (unsigned)(uintptr_t)smem + K2_RING + gw * 24576;
        f32x16 ac0, zr0; f32x8 ac1, zr1; f32x4 ac2, zr2; f32x2 rb; u32x4 zo;
        f32x32 wb0, wb1, wb2; f32x16 wb3; unsigned lo = (tid & 63) * 16;
        {
            const int lane = tid & 63, l16 = lane & 15, g4 = lane >> 4;
            u32x8 av;
#pragma unroll
            for (int i = 0; i < 8; ++i) av[i] = ((gw ^ (i >> 1)) * 16 + 8 * (i & 1) + (lane >> 3)) * 4096 + 16 * ((lane & 7) ^ (4 * (i & 1) + g4));
            const char* abase = (const char*)(wsp + (size_t)(tt * 64) * 2048 + gw * 512);
            const char* bbase = ws + WS_Z2 + (size_t)(ft * 64 + gw * 16) * 7168 + 3072;
            const unsigned ayb = ring + l16 * 128;
            const unsigned po = (tt * 2 + (gw >> 1)) * (56 * 1024) + (g4 >> 1) * 1024 + (16 * (gw & 1) + l16 + 32 * (g4 & 1)) * 16;
            const unsigned t0 = (7 * ft + 1) >> 1;
#pragma unroll
            for (int k = 0; k < 3; ++k) zo[k] = (unsigned)WS_Z1 + po + (t0 + k) * 2048;
            zo[3] = (unsigned)WS_Z1 + po + ((ft & 1) ? (t0 - 1) * 2048 + 8 : (t0 + 3) * 2048);
            const char* b0 = ws + WS_WC + (size_t)((oct * 2 + (wi & 1)) * 88 + 32 + 14 * wi + 4) * 1024;
            const char* b1 = ws + WS_WC + (size_t)((oct * 2 + (wi & 1 ^ 1)) * 88 + 32 + 14 * wi + 4) * 1024;

            asm volatile(GEMM_ASM
                : "={v[0:15]}"(ac0), "={v[16:23]}"(ac1), "={v[24:27]}"(ac2),
                  "={v[212:227]}"(zr0), "={v[228:235]}"(zr1), "={v[236:239]}"(zr2), "={v[240:241]}"(rb), "+{v208}"(lo),
                  "={v[64:95]}"(wb0), "={v[96:127]}"(wb1), "={v[128:159]}"(wb2), "={v[168:183]}"(wb3),
                  "+{s[40:41]}"(abase), "+{s[42:43]}"(bbase), "+{s[48:49]}"(b0), "+{s[50:51]}"(b1)
                : "{v[200:207]}"(av), "{v209}"(ring + lane * 16),
                  "{v210}"(ayb + 16 * (g4 ^ (l16 >> 1))), "{v211}"(ayb + 16 * ((4 + g4) ^ (l16 >> 1))),
                  "{v[248:251]}"(zo), "{v252}"((unsigned)WS_RS + (tt * 64 + gw * 16 + l16) * 8),
                  [ring] "s"(ring), [ws] "s"(ws)
                : "memory", "scc", GEMM_CLOB);
        }

        const int lane = lo >> 4;
        group_arrive((LAS unsigned*)(smem + K2_CNT + 4), lane);
        float g[28], zr[28];
#pragma unroll
        for (int k = 0; k < 28; ++k) {
            g[k] = fadd1(k < 16 ? ac0[k & 15] : k < 24 ? ac1[k & 7] : ac2[k & 3], rb[1]);
            zr[k] = k < 16 ? zr0[k & 15] : k < 24 ? zr1[k & 7] : zr2[k & 3];
        }
        SBAR();

        group_wait((volatile LAS unsigned*)(smem + K2_CNT + 4));

        f32x4 pv[3][7];
#define RD_PV(s) _Pragma("unroll") for (int i = 0; i < 7; ++i) pv[s][i] = *(const f32x4*)(smem + K2_RING + (gw ^ (1 + (s))) * 24576 + (7 * (s) + i) * 1024 + lo)
#define AD_PV(s) _Pragma("unroll") for (int k = 0; k < 28; ++k) g[k] = fadd1(g[k], pv[s][k >> 2][k & 3])
        RD_PV(0); RD_PV(1); SBAR();
        AD_PV(0); SBAR();
        RD_PV(2); SBAR();
        AD_PV(1); SBAR();
        AD_PV(2); SBAR();
#pragma unroll
        for (int k = 0; k < 28; ++k) g[k] = fmul1(g[k], zr[k]);
        SBAR();
        unsigned uo[14];
#pragma unroll
        for (int k = 0; k < 14; ++k) uo[k] = pk2(g[2 * k], g[2 * k + 1]);
        {
            const char* cb = ws + WS_CAT + (size_t)((tt * 2 + (gw >> 1)) * 88 + 32 + 7 * ft + 2) * 1024;
            const unsigned l16 = lane & 15, g4 = lane >> 4, rw = (16 * (gw & 1) + l16) * 16;
            asm volatile("global_store_dwordx4 %0, %2, %6 offset:-2048 sc1\n\tglobal_store_dwordx4 %0, %3, %6 sc1\n\tglobal_store_dwordx4 %0, %4, %6 offset:2048 sc1\n\t"
                         "global_store_dwordx2 %1, %5, %6 sc1\n\ts_nop 1"
                         :: "v"(rw + g4 * 512), "v"(4096 + rw + (g4 >> 1) * 512 + (g4 & 1) * 8), "v"(u32x4{uo[0], uo[1], uo[2], uo[3]}), "v"(u32x4{uo[4], uo[5], uo[6], uo[7]}),
                            "v"(u32x4{uo[8], uo[9], uo[10], uo[11]}), "v"(u32x2{uo[12], uo[13]}), "s"(cb) : "memory");
        }
        {
            const char* a0 = ws + WS_CAT + (size_t)((ort * 2 + (wi >> 1)) * 88 + 32 + 14 * wi + 4) * 1024;
            const char* a1 = ws + WS_CAT + (size_t)((ort * 2 + (wi >> 1 ^ 1)) * 88 + 32 + 14 * wi + 4) * 1024;
            f32x8 mine; u32x4 xr;
            const unsigned xo = (ort * 64 + (wi >> 1) * 32 + (lane & 31)) * 896 + (oct * 64 + (wi & 1) * 32 + 16 + 8 * (lane >> 5)) * 2;
            int cnt;
            asm volatile(P3G_MAIN
                : "={v[8:15]}"(mine), [xr] "=&{v[240:243]}"(xr), "+{s[44:45]}"(a0), "+{s[46:47]}"(a1), [cnt] "=&s"(cnt)
                : "{v[64:95]}"(wb0), "{v[96:127]}"(wb1), "{v[128:159]}"(wb2), "{v[168:183]}"(wb3), [vo] "{v248}"(lo), [xo] "{v249}"(xo), [xb] "s"(x), [ws] "s"(ws),
                  [fo] "{v250}"((unsigned)WS_FL + (tt * 64 + 32 + ft * 4 + gw) * FLS),
                  [act] "s"(idx), [msk] "s"(0xffu << (8 * wi)), [lc] "s"(lds0 + K2_CNT + 8), [w0] "{v252}"(lds0 + K2_RING + (wi ^ 1) * 24576 + 4096 + lo),
                  [w1] "{v253}"(lds0 + K2_RING + (wi ^ 1) * 24576 + lo), [w2] "{v254}"(lds0 + K2_RING + (wi ^ 2) * 24576 + 7168 + lo),
                  [w3] "{v255}"(lds0 + K2_RING + (wi ^ 3) * 24576 + 14336 + lo)
                : "memory", "scc", "vcc", P3G_CLOB);
            finish_out<1>(smem, wi, lo, mine, xr, (char*)out + xo);
            __builtin_amdgcn_endpgm();
        }
    }
}

extern "C" void kernel_launch(void* const* d_in, const int* in_sizes, int n_in, void* d_out, int out_size, void* d_ws, size_t ws_size,
                              hipStream_t stream) {
    const u16* x = (const u16*)d_in[0]; const u16* wqkv = (const u16*)d_in[1]; const u16* wo = (const u16*)d_in[2];
    const u16* win = (const u16*)d_in[4]; const u16* wsp = (const u16*)d_in[5]; const u16* bsp = (const u16*)d_in[6];
    const u16* wout = (const u16*)d_in[7];
    char* ws = (char*)d_ws;
    (void)hipFuncSetAttribute((const void*)k1_proj, hipFuncAttributeMaxDynamicSharedMemorySize, K1_LDS);
    (void)hipFuncSetAttribute((const void*)k2_mix, hipFuncAttributeMaxDynamicSharedMemorySize, K2_LDS);
    k1_proj<<<256, 512, K1_LDS, stream>>>(x, wqkv, win, wo, wout, wsp, bsp, ws);
    k2_mix<<<256, 512, K2_LDS, stream>>>(wsp, x, ws, (u16*)d_out);
}
```
